```python
import torch

def _softmax(x, axis=-1):
    return torch.softmax(x, dim=axis)
def _gelu(x):
    return torch.nn.functional.gelu(x, approximate="tanh")
def _silu(x):
    return torch.nn.functional.silu(x)
def _sigmoid(x):
    return torch.sigmoid(x)
def _layernorm(x, axis=-1, eps=1e-5):
    m = x.mean(dim=axis, keepdim=True)
    v = x.var(dim=axis, keepdim=True, unbiased=False)
    return (x - m) / torch.sqrt(v + eps)
def _scatter_add(tgt, idx, src, axis=0):
    return tgt.clone().index_add_(axis, idx, src)
def _segment_sum(src, seg, n):
    out = torch.zeros((n,) + src.shape[1:], dtype=src.dtype)
    return out.index_add_(0, seg, src)
def _pad(x, pw):
    flat = tuple(v for pair in reversed(pw) for v in pair)
    return torch.nn.functional.pad(x, flat)

kernel_name = 'standalone__sliding_block__prompt'

def setup_inputs():

    _w = (lambda a: (lambda t: t.to(torch.bfloat16) if t.is_floating_point() else t)(torch.as_tensor(a)))
    B, S, D = 1, 2048, 512
    return {
        "x": _w((lambda *s: torch.randn(*s, dtype=torch.bfloat16) * 0.4)(B, S, D)),
        "wqkv": _w((lambda *s: torch.randn(*s, dtype=torch.bfloat16) * 0.4)(D, 3 * D)),
        "wo": _w((lambda *s: torch.randn(*s, dtype=torch.bfloat16) * 0.05)(D, D)),
    }

def reference(x, wqkv, wo):
    B, S, D = x.shape
    H = 32; Dh = D // H; W = 256
    qkv = (lambda x, *d: x.permute(*d))((x @ wqkv).reshape(B, S, 3, H, Dh), 2, 0, 3, 1, 4)
    q, k, v = qkv[0], qkv[1], qkv[2]
    scores = torch.einsum("bhqd,bhkd->bhqk", q, k) * (Dh ** -0.5)
    idx = torch.arange(S)
    mask = (idx[:, None] - idx[None, :] >= 0) & (idx[:, None] - idx[None, :] < W)
    scores = torch.where(mask[None, None], scores, -1e9)
    p = _softmax(scores, axis=-1)
    o = torch.einsum("bhqk,bhkd->bhqd", p, v)
    return (lambda x, *d: x.permute(*d))(o, 0, 2, 1, 3).reshape(B, S, D) @ wo
```

```cpp
#include <hip/hip_runtime.h>
#include <hip/hip_bf16.h>

#define ROUND_SCORES 0

static constexpr int S = 2048, D = 512, H = 32, DH = 16, W = 256;

__device__ __forceinline__ float bf2f(__hip_bfloat16 v) { return __bfloat162float(v); }
__device__ __forceinline__ float rbf(float v) { return __bfloat162float(__float2bfloat16(v)); }

__global__ void gemm_naive(const __hip_bfloat16* A, const __hip_bfloat16* B, __hip_bfloat16* C, int M, int N, int K) {
    int n = blockIdx.x * blockDim.x + threadIdx.x;
    int m = blockIdx.y;
    if (n >= N) return;
    float acc = 0.f;
    for (int k = 0; k < K; ++k) acc += bf2f(A[m * K + k]) * bf2f(B[k * N + n]);
    C[m * N + n] = __float2bfloat16(acc);
}

__global__ void attn_naive(const __hip_bfloat16* qkv, __hip_bfloat16* o) {
    int i = blockIdx.x * blockDim.x + threadIdx.x;
    int h = blockIdx.y;
    float q[DH];
    for (int d = 0; d < DH; ++d) q[d] = bf2f(qkv[i * 3 * D + h * DH + d]);
    int j0 = i - W + 1; if (j0 < 0) j0 = 0;
    float m = -1e30f;
    for (int j = j0; j <= i; ++j) {
        float s = 0.f;
        for (int d = 0; d < DH; ++d) s += q[d] * bf2f(qkv[j * 3 * D + D + h * DH + d]);
        s *= 0.25f;
        m = fmaxf(m, s);
    }
    float l = 0.f;
    for (int j = j0; j <= i; ++j) {
        float s = 0.f;
        for (int d = 0; d < DH; ++d) s += q[d] * bf2f(qkv[j * 3 * D + D + h * DH + d]);
        s *= 0.25f;
        l += __expf(s - m);
    }
    float acc[DH];
    for (int d = 0; d < DH; ++d) acc[d] = 0.f;
    for (int j = j0; j <= i; ++j) {
        float s = 0.f;
        for (int d = 0; d < DH; ++d) s += q[d] * bf2f(qkv[j * 3 * D + D + h * DH + d]);
        s *= 0.25f;
        float p = __expf(s - m) / l;
        for (int d = 0; d < DH; ++d) acc[d] += p * bf2f(qkv[j * 3 * D + 2 * D + h * DH + d]);
    }
    for (int d = 0; d < DH; ++d) o[i * D + h * DH + d] = __float2bfloat16(acc[d]);
}

extern "C" void kernel_launch(void* const* d_in, const int* in_sizes, int n_in,
                              void* d_out, int out_size, void* d_ws, size_t ws_size,
                              hipStream_t stream) {
    const __hip_bfloat16* x = (const __hip_bfloat16*)d_in[0];
    const __hip_bfloat16* wqkv = (const __hip_bfloat16*)d_in[1];
    const __hip_bfloat16* wo = (const __hip_bfloat16*)d_in[2];
    __hip_bfloat16* out = (__hip_bfloat16*)d_out;
    __hip_bfloat16* qkv = (__hip_bfloat16*)d_ws;
    __hip_bfloat16* o = qkv + S * 3 * D;
    gemm_naive<<<dim3(3 * D / 256, S), 256, 0, stream>>>(x, wqkv, qkv, S, 3 * D, D);
    attn_naive<<<dim3(S / 64, H), 64, 0, stream>>>(qkv, o);
    gemm_naive<<<dim3(D / 256, S), 256, 0, stream>>>(o, wo, out, S, D, D);
}
```

```cpp
#include <hip/hip_runtime.h>
#include <hip/hip_bf16.h>
#include <stdint.h>

typedef __attribute__((ext_vector_type(8))) short bf16x8;
typedef __attribute__((ext_vector_type(4))) short s16x4;
typedef __attribute__((ext_vector_type(16))) float f32x16;
typedef __attribute__((ext_vector_type(4))) float f32x4;
typedef __attribute__((ext_vector_type(2))) float f32x2;
typedef __attribute__((ext_vector_type(4))) unsigned u32x4;
typedef __attribute__((ext_vector_type(2))) __bf16 bf16x2;
typedef unsigned long long u64;
#define LAS __attribute__((address_space(3)))
#define DI __device__ __forceinline__
typedef LAS char* lptr;

constexpr unsigned WS_K = 0, WS_V = 2u << 20, WS_O = 4u << 20, WS_G = 6u << 20, WS_F = WS_G + 256 * 128, WS_END = WS_F + 256 * 128;
constexpr unsigned WS_DBG = 8u << 20;
constexpr unsigned SLOT = 16384;
constexpr unsigned L_X = 0;
constexpr unsigned L_W = 4 * SLOT;
constexpr unsigned L_R = 3 * SLOT;
constexpr unsigned L_M = L_W + 3 * SLOT;
constexpr unsigned L_E = 2 * SLOT;
constexpr unsigned LDS_BYTES = 10 * SLOT;
constexpr int MAXSPIN = 1 << 15;
constexpr float QSCALE = 0.25f * 1.4426950408889634f;

#define MFMA(a, b, c) __builtin_amdgcn_mfma_f32_32x32x16_bf16((a), (b), (c), 0, 0, 0)
#define WAIT_BAR(N) asm volatile("s_waitcnt vmcnt(" #N ") lgkmcnt(0)\n\ts_barrier" ::: "memory")
#define BAR() asm volatile("s_waitcnt lgkmcnt(0)\n\ts_barrier" ::: "memory")
#define MX3(a, b, c) __builtin_fmaxf(__builtin_fmaxf((a), (b)), (c))

#define STAMP(i) do {} while (0)

DI unsigned pk(float a, float b) { f32x2 v = {a, b}; bf16x2 r = __builtin_convertvector(v, bf16x2); return __builtin_bit_cast(unsigned, r); }
DI unsigned nosent(unsigned x) { return x < 0xFFFFFFFEu ? x : 0xFFFFFFFEu; }
DI void glds16(unsigned voff, const char* sbase, unsigned lds_dst) {
    unsigned sv;
    asm volatile("s_mov_b32 %0, m0\n\ts_mov_b32 m0, %3\n\ts_nop 0\n\tglobal_load_lds_dwordx4 %1, %2\n\ts_mov_b32 m0, %0"
                 : "=&s"(sv) : "v"(voff), "s"(sbase), "s"(lds_dst) : "memory");
}
DI s16x4 trd(lptr p) { return __builtin_amdgcn_ds_read_tr16_b64_v4i16((LAS s16x4*)p); }
DI bf16x8 trd2(lptr p, int off2) { s16x4 a = trd(p), b = trd(p + off2); return __builtin_shufflevector(a, b, 0, 1, 2, 3, 4, 5, 6, 7); }
DI u64 hsh(u64 g, unsigned slot) { return g * 0x9E3779B97F4A7C15ull + (u64)(2u * slot + 1u) * 0xD1B54A32D192ED03ull; }
DI bf16x8 asbf(u32x4 v) { return __builtin_bit_cast(bf16x8, v); }
DI unsigned umax4(u32x4 v) { unsigned a = v.x > v.y ? v.x : v.y, b = v.z > v.w ? v.z : v.w; return a > b ? a : b; }
DI unsigned umax2(unsigned a, unsigned b) { return a > b ? a : b; }
DI float max16(const f32x16& s, float m) {
#pragma unroll
    for (int i = 0; i < 16; i += 2) m = MX3(m, s[i], s[i + 1]);
    return m;
}
DI float hmerge(float m) { auto rr = __builtin_amdgcn_permlane32_swap(__float_as_uint(m), __float_as_uint(m), false, false);
    return __builtin_fmaxf(__uint_as_float(rr[0]), __uint_as_float(rr[1])); }
constexpr int crow(int r, int hi) { return (r & 3) + 8 * (r >> 2) + 4 * hi; }
constexpr u64 diag_mask(int reg) { u64 m = 0; for (int l = 0; l < 64; ++l) if (crow(reg, l >> 5) <= (l & 31)) m |= 1ull << l; return m; }

DI void pv_tile(const f32x16& s, const u32x4& v0, const u32x4& v1, f32x16& o) {
    u32x4 p0, p1;
#pragma unroll
    for (int i = 0; i < 4; ++i) {
        p0[i] = pk(__builtin_amdgcn_exp2f(s[2 * i]), __builtin_amdgcn_exp2f(s[2 * i + 1]));
    }
    o = MFMA(asbf(v0), asbf(p0), o);
#pragma unroll
    for (int i = 0; i < 4; ++i) {
        p1[i] = pk(__builtin_amdgcn_exp2f(s[8 + 2 * i]), __builtin_amdgcn_exp2f(s[8 + 2 * i + 1]));
    }
    o = MFMA(asbf(v1), asbf(p1), o);
}

__global__ void __launch_bounds__(512, 2)
fused_kernel(const char* __restrict__ x, const char* __restrict__ wqkv, const char* __restrict__ wo, char* __restrict__ out, char* ws) {
    extern __shared__ __attribute__((aligned(16))) char smem[];
    const lptr lds = (lptr)smem;
    const unsigned lds0 = (unsigned)(uintptr_t)smem;
    const int tid = threadIdx.x, lane = tid & 63;
    const int w = __builtin_amdgcn_readfirstlane(tid >> 6);
    const int r = lane & 31, hh = lane >> 5;
    const int id = blockIdx.x, xcd = id & 7, idx = id >> 3;
    const int b = 8 * (xcd >> 1) + (idx >> 2), g = 4 * (xcd & 1) + (idx & 3);
    const int mt = w & 1, j = (w >> 1) & 1, kh = w >> 2;
    const int head = 4 * g + 2 * j + kh;
    const int qo = 2 * b + mt;
    const int self = b * 8 + g;
    const __amdgpu_buffer_rsrc_t rs = __builtin_amdgcn_make_buffer_rsrc(ws, 0, WS_END, 0x00020000);
    STAMP(0);

    const int cons = lane < 4 ? (b + 1 + lane) * 8 + g : b * 8 + ((lane - 4) & 7);
    const bool cons_ok = lane < 4 ? (b + 1 + lane < 32) : (lane < 12);
    const unsigned cslot = lane < 4 ? lane : 4 + g;
    u64 gcons, gself;
    {
        const char* pc = ws + WS_G + (cons_ok ? cons : self) * 128;
        const char* ps = ws + WS_G + self * 128;
        asm volatile("global_load_dwordx2 %0, %1, off sc1" : "=v"(gcons) : "v"(pc) : "memory");
        asm volatile("global_load_dwordx2 %0, %1, off sc1" : "=v"(gself) : "v"(ps) : "memory");
    }
    const unsigned offK = WS_K + (unsigned)(head * 64 + qo) * 1024 + lane * 16;
    const unsigned offV = WS_V + (unsigned)(head * 64 + qo) * 1024 + (hh * 16 + (r & 15)) * 16;
    const unsigned offO = WS_O + (unsigned)(qo * 32 + head) * 1024 + lane * 16;
    {
        const u32x4 sent = {~0u, ~0u, ~0u, ~0u};
        __builtin_amdgcn_raw_buffer_store_b128(sent, rs, offK, 0, 16);
        if (r < 16) {
            __builtin_amdgcn_raw_buffer_store_b128(sent, rs, offV, 0, 16);
            __builtin_amdgcn_raw_buffer_store_b128(sent, rs, offV + 512, 0, 16);
        }
        __builtin_amdgcn_raw_buffer_store_b128(sent, rs, offO, 0, 16);
    }
    {
        const char* xb = x + (size_t)(64 * b + 8 * w) * 1024;
#pragma unroll
        for (int i = 0; i < 8; ++i) {
            const unsigned row15 = (unsigned)((8 * w + i) & 15);
            glds16(((unsigned)lane ^ row15) * 16u, xb + i * 1024, lds0 + L_X + (unsigned)(8 * w + i) * 1024);
        }
    }
    const unsigned voffW = (unsigned)(lane >> 3) * 3072u + (unsigned)((lane & 7) ^ (((lane >> 4) & 1) << 2)) * 16u;
    const char* wb = wqkv + (size_t)(16 * w) * 3072 + g * 128;
#define DMA_W(c, slotoff) do { \
        const char* s_ = wb + (size_t)(128 * ((c) & 3)) * 3072 + ((c) < 4 ? 1024 : ((c) < 8 ? 2048 : 0)); \
        glds16(voffW, s_, lds0 + (slotoff) + (unsigned)w * 2048); \
        glds16(voffW, s_ + 8 * 3072, lds0 + (slotoff) + (unsigned)w * 2048 + 1024); } while (0)
    DMA_W(0, L_W + 0 * SLOT); DMA_W(1, L_W + 1 * SLOT); DMA_W(2, L_W + 2 * SLOT);
    DMA_W(3, L_W + 3 * SLOT); DMA_W(4, L_W + 4 * SLOT); DMA_W(5, L_W + 5 * SLOT);
    asm volatile("s_waitcnt vmcnt(20)\n\ts_barrier" : "+v"(gcons), "+v"(gself) :: "memory");
    if (w == 0 && cons_ok) {
        u64* pf = (u64*)(ws + WS_F + cons * 128 + cslot * 8);
        const u64 val = hsh(gcons, cslot);
        asm volatile("global_store_dwordx2 %0, %1, off sc1" :: "v"(pf), "v"(val) : "memory");
    }
    STAMP(1);
    WAIT_BAR(12);
    STAMP(2);
    bf16x8 xf[16];
    {
        const int row = 32 * mt + r;
        const lptr xr = lds + L_X + row * 1024;
#pragma unroll
        for (int t = 0; t < 16; ++t) {
            const int ch = (4 * (t & 3) + 2 * kh + hh) ^ (row & 15);
            xf[t] = *(LAS bf16x8*)(xr + (t >> 2) * 256 + ch * 16);
        }
    }
    BAR();
    DMA_W(6, L_X + 0 * SLOT); DMA_W(7, L_X + 1 * SLOT); DMA_W(8, L_X + 2 * SLOT);

    const int blk = ((lane >> 4) & 1) ^ kh, q4 = (lane & 15) >> 2, p4 = lane & 3;
    const lptr wr = lds + (8 * hh + q4) * 128 + 64 * (j ^ (q4 >> 1)) + 32 * blk + 8 * p4 + kh * 2048;
    f32x16 acc;
#define STEP(slotoff, kc, SWAP) do { \
        bf16x8 wf_[4]; \
        _Pragma("unroll") for (int u = 0; u < 4; ++u) wf_[u] = trd2(wr + (slotoff) + u * 4096, 512); \
        _Pragma("unroll") for (int u = 0; u < 4; ++u) acc = SWAP ? MFMA(xf[4 * (kc) + u], wf_[u], acc) : MFMA(wf_[u], xf[4 * (kc) + u], acc); \
    } while (0)
    const lptr rw = lds + L_R + w * 2048 + lane * 16;
    const lptr rp = lds + L_R + (w ^ 4) * 2048 + lane * 16;

    u32x4 kf[5], vf0[5], vf1[5];
    acc = f32x16{};
    WAIT_BAR(16); STEP(L_W + 0 * SLOT, 0, false);
    WAIT_BAR(14); DMA_W(9, L_W + 0 * SLOT); STEP(L_W + 1 * SLOT, 1, false);
    WAIT_BAR(14); DMA_W(10, L_W + 1 * SLOT); STEP(L_W + 2 * SLOT, 2, false);
    WAIT_BAR(14); DMA_W(11, L_W + 2 * SLOT); STEP(L_W + 3 * SLOT, 3, false);
    *(LAS f32x4*)(rw) = f32x4{acc[8], acc[9], acc[10], acc[11]};
    *(LAS f32x4*)(rw + 1024) = f32x4{acc[12], acc[13], acc[14], acc[15]};
    BAR();
    {
        const f32x4 a = *(LAS f32x4*)(rp), c = *(LAS f32x4*)(rp + 1024);
        u32x4 k4;
        k4[0] = pk(acc[0] + a[0], acc[1] + a[1]); k4[1] = pk(acc[2] + a[2], acc[3] + a[3]);
        k4[2] = pk(acc[4] + c[0], acc[5] + c[1]); k4[3] = pk(acc[6] + c[2], acc[7] + c[3]);
#pragma unroll
        for (int i = 0; i < 4; ++i) k4[i] = nosent(k4[i]);
        kf[4] = k4;
        __builtin_amdgcn_raw_buffer_store_b128(k4, rs, offK, 0, 16);
    }
    STAMP(3);
    acc = f32x16{};
    WAIT_BAR(15); STEP(L_W + 4 * SLOT, 0, true);
    WAIT_BAR(13); STEP(L_W + 5 * SLOT, 1, true);
    WAIT_BAR(11); STEP(L_X + 0 * SLOT, 2, true);
    WAIT_BAR(9); STEP(L_X + 1 * SLOT, 3, true);
    {
        const int li = hh * 16 + (r & 15);
        if (r >= 16) {
            const lptr d = lds + L_R + w * 2048 + li * 16;
#pragma unroll
            for (int i = 0; i < 4; ++i) *(LAS f32x4*)(d + i * 512) = f32x4{acc[4 * i], acc[4 * i + 1], acc[4 * i + 2], acc[4 * i + 3]};
        }
        BAR();
        u32x4 a0 = {0x3F803F80u, 0x3F803F80u, 0x3F803F80u, 0x3F803F80u}, a1 = a0;
        if (r < 16) {
            const lptr s = lds + L_R + (w ^ 4) * 2048 + li * 16;
            f32x4 pp[4];
#pragma unroll
            for (int i = 0; i < 4; ++i) pp[i] = *(LAS f32x4*)(s + i * 512);
#pragma unroll
            for (int i = 0; i < 2; ++i) {
                a0[2 * i] = nosent(pk(acc[4 * i] + pp[i][0], acc[4 * i + 1] + pp[i][1]));
                a0[2 * i + 1] = nosent(pk(acc[4 * i + 2] + pp[i][2], acc[4 * i + 3] + pp[i][3]));
                a1[2 * i] = nosent(pk(acc[8 + 4 * i] + pp[2 + i][0], acc[8 + 4 * i + 1] + pp[2 + i][1]));
                a1[2 * i + 1] = nosent(pk(acc[8 + 4 * i + 2] + pp[2 + i][2], acc[8 + 4 * i + 3] + pp[2 + i][3]));
            }
            __builtin_amdgcn_raw_buffer_store_b128(a0, rs, offV, 0, 16);
            __builtin_amdgcn_raw_buffer_store_b128(a1, rs, offV + 512, 0, 16);
        }
        vf0[4] = a0; vf1[4] = a1;
    }
    STAMP(4);
    u64 fw;
    const char* pfs = ws + WS_F + self * 128 + (lane < 12 ? lane : 0) * 8;
    acc = f32x16{};
    WAIT_BAR(9);
    asm volatile("global_load_dwordx2 %0, %1, off sc1" : "=v"(fw) : "v"(pfs) : "memory");
    STEP(L_X + 2 * SLOT, 0, false);
    WAIT_BAR(8); STEP(L_W + 0 * SLOT, 1, false);
    WAIT_BAR(6); STEP(L_W + 1 * SLOT, 2, false);
    asm volatile("s_waitcnt vmcnt(0) lgkmcnt(0)\n\ts_barrier" : "+v"(fw) :: "memory");
    const int i0 = b < 4 ? 4 - b : 0;
    {
        const bool need = lane < 4 ? (b - 1 - lane >= 0) : (lane < 12);
        const u64 want = hsh(gself, lane < 12 ? lane : 0);
        int spins = 0;
        while (__any(need && fw != want) && ++spins < MAXSPIN) {
            __builtin_amdgcn_s_sleep(2);
            asm volatile("global_load_dwordx2 %0, %1, off sc1\n\ts_waitcnt vmcnt(0)" : "=v"(fw) : "v"(pfs) : "memory");
        }
    }
    const unsigned rK = offK - 8 * 1024, rV = offV - 8 * 1024;
#pragma unroll
    for (int i = 0; i < 4; ++i) {
        kf[i] = u32x4{0, 0, 0, 0};
        if (i >= i0) kf[i] = __builtin_amdgcn_raw_buffer_load_b128(rs, rK + i * 2048, 0, 16);
    }
    STEP(L_W + 2 * SLOT, 3, false);
    {
        const lptr mw = lds + L_M + w * 2048 + lane * 16;
        *(LAS f32x4*)(rw) = f32x4{acc[0], acc[1], acc[2], acc[3]};
        *(LAS f32x4*)(rw + 1024) = f32x4{acc[4], acc[5], acc[6], acc[7]};
        *(LAS f32x4*)(mw) = f32x4{acc[8], acc[9], acc[10], acc[11]};
        *(LAS f32x4*)(mw + 1024) = f32x4{acc[12], acc[13], acc[14], acc[15]};
    }
    BAR();
    bf16x8 qfo, qfx;
    {
        const lptr m4 = lds + L_M + (w ^ 4) * 2048 + lane * 16;
        const lptr r1 = lds + L_R + (w ^ 1) * 2048 + lane * 16;
        const lptr m5 = lds + L_M + (w ^ 5) * 2048 + lane * 16;
        const f32x4 a = *(LAS f32x4*)(m4), c = *(LAS f32x4*)(m4 + 1024);
        const f32x4 d0 = *(LAS f32x4*)(r1), d1 = *(LAS f32x4*)(r1 + 1024);
        const f32x4 e0 = *(LAS f32x4*)(m5), e1 = *(LAS f32x4*)(m5 + 1024);
        u32x4 t;
        t[0] = pk((acc[0] + a[0]) * QSCALE, (acc[1] + a[1]) * QSCALE); t[1] = pk((acc[2] + a[2]) * QSCALE, (acc[3] + a[3]) * QSCALE);
        t[2] = pk((acc[4] + c[0]) * QSCALE, (acc[5] + c[1]) * QSCALE); t[3] = pk((acc[6] + c[2]) * QSCALE, (acc[7] + c[3]) * QSCALE);
        qfo = asbf(t);
        t[0] = pk((d0[0] + e0[0]) * QSCALE, (d0[1] + e0[1]) * QSCALE); t[1] = pk((d0[2] + e0[2]) * QSCALE, (d0[3] + e0[3]) * QSCALE);
        t[2] = pk((d1[0] + e1[0]) * QSCALE, (d1[1] + e1[1]) * QSCALE); t[3] = pk((d1[2] + e1[2]) * QSCALE, (d1[3] + e1[3]) * QSCALE);
        qfx = asbf(t);
    }
#pragma unroll
    for (int i = 0; i < 4; ++i) {
        vf0[i] = u32x4{0x3F803F80u, 0x3F803F80u, 0x3F803F80u, 0x3F803F80u}; vf1[i] = vf0[i];
        if (i >= i0 && r < 16) {
            vf0[i] = __builtin_amdgcn_raw_buffer_load_b128(rs, rV + i * 2048, 0, 16);
            vf1[i] = __builtin_amdgcn_raw_buffer_load_b128(rs, rV + i * 2048 + 512, 0, 16);
        }
    }
    STAMP(5);
    const f32x16 zero = {};
    const float NINF = -__builtin_inff();
    float mo = NINF, mx = NINF;
    f32x16 sc = MFMA(asbf(kf[4]), qfo, zero);
    if (mt == 0) { const f32x16 s = MFMA(asbf(kf[4]), qfx, zero); mx = max16(s, mx); }
    {
        int spins = 0;
        while (__any(umax2(umax2(umax4(kf[0]), umax4(kf[1])), umax2(umax4(kf[2]), umax4(kf[3]))) == ~0u) && ++spins < MAXSPIN) {
            __builtin_amdgcn_s_sleep(1);
#pragma unroll
            for (int i = 0; i < 4; ++i) if (i >= i0) kf[i] = __builtin_amdgcn_raw_buffer_load_b128(rs, rK + i * 2048, 0, 16);
        }
    }
    STAMP(6);
    {
        f32x16 sf;
        if (i0 == 0) sf = MFMA(asbf(kf[0]), qfo, zero);
        else {
#pragma unroll
            for (int i = 0; i < 16; ++i) sf[i] = NINF;
        }
#pragma unroll
        for (int i = 0; i < 16; ++i) sc[i] = __builtin_amdgcn_inverse_ballot_w64(diag_mask(i)) ? sc[i] : sf[i];
        mo = max16(sc, mo);
    }
    if (mt == 1 && i0 == 0) { const f32x16 s = MFMA(asbf(kf[0]), qfx, zero); mx = max16(s, mx); }
#pragma unroll
    for (int i = 1; i < 4; ++i) {
        if (i >= i0) {
            const f32x16 s1 = MFMA(asbf(kf[i]), qfo, zero), s2 = MFMA(asbf(kf[i]), qfx, zero);
            mo = max16(s1, mo); mx = max16(s2, mx);
        }
    }
    mo = hmerge(mo); mx = hmerge(mx);
    *(LAS f32x2*)(lds + L_E + w * 512 + lane * 8) = f32x2{mo, mx};
    {
        int spins = 0;
        for (;;) {
            unsigned m = 0;
#pragma unroll
            for (int i = 0; i < 4; ++i) m = umax2(m, umax2(umax4(vf0[i]), umax4(vf1[i])));
            if (!__any(m == ~0u) || ++spins >= MAXSPIN) break;
            __builtin_amdgcn_s_sleep(1);
#pragma unroll
            for (int i = 0; i < 4; ++i) if (i >= i0 && r < 16) {
                vf0[i] = __builtin_amdgcn_raw_buffer_load_b128(rs, rV + i * 2048, 0, 16);
                vf1[i] = __builtin_amdgcn_raw_buffer_load_b128(rs, rV + i * 2048 + 512, 0, 16);
            }
        }
    }
    BAR();
    STAMP(7);
    const int cb = g & 3;
    {
        const unsigned voffO = (unsigned)(lane >> 4) * 1024u + (unsigned)((lane & 15) ^ (((lane >> 4) & 3) << 2)) * 16u;
        const char* ob = wo + (size_t)(8 * w) * 1024 + cb * 256;
#pragma unroll
        for (int c = 0; c < 8; ++c) {
            const unsigned so = (c < 6 ? L_W + c * SLOT : L_X + (c - 6) * SLOT) + (unsigned)w * 2048;
            glds16(voffO, ob + (size_t)(64 * c) * 1024, lds0 + so);
            glds16(voffO, ob + (size_t)(64 * c + 4) * 1024, lds0 + so + 1024);
        }
    }
    {
        const f32x2 pm = *(LAS f32x2*)(lds + L_E + (w ^ 1) * 512 + lane * 8);
        mo = __builtin_fmaxf(mo, pm[1]); mx = __builtin_fmaxf(mx, pm[0]);
    }
    {
        f32x16 nm;
#pragma unroll
        for (int i = 0; i < 16; ++i) nm[i] = -mx;
        f32x16 o = {};
        if (mt == 0) { const f32x16 s = MFMA(asbf(kf[4]), qfx, nm); pv_tile(s, vf0[4], vf1[4], o); }
        else if (i0 == 0) { const f32x16 s = MFMA(asbf(kf[0]), qfx, nm); pv_tile(s, vf0[0], vf1[0], o); }
#pragma unroll
        for (int i = 1; i < 4; ++i) if (i >= i0) { const f32x16 s = MFMA(asbf(kf[i]), qfx, nm); pv_tile(s, vf0[i], vf1[i], o); }
        const lptr d = lds + L_E + 4096 + w * 3072 + lane * 16;
        *(LAS f32x4*)(d) = f32x4{o[0], o[1], o[2], o[3]};
        *(LAS f32x4*)(d + 1024) = f32x4{o[4], o[5], o[6], o[7]};
        *(LAS float*)(lds + L_E + 4096 + w * 3072 + 2048 + lane * 4) = o[8];
    }
    f32x16 o = {};
    {
        f32x16 nm;
#pragma unroll
        for (int i = 0; i < 16; ++i) nm[i] = -mo;
#pragma unroll
        for (int i = 1; i < 4; ++i) if (i >= i0) { const f32x16 s = MFMA(asbf(kf[i]), qfo, nm); pv_tile(s, vf0[i], vf1[i], o); }
        u32x4 p0, p1, d0, d1;
#pragma unroll
        for (int i = 0; i < 4; ++i) {
            p0[i] = pk(__builtin_amdgcn_exp2f(sc[2 * i] - mo), __builtin_amdgcn_exp2f(sc[2 * i + 1] - mo));
            p1[i] = pk(__builtin_amdgcn_exp2f(sc[8 + 2 * i] - mo), __builtin_amdgcn_exp2f(sc[8 + 2 * i + 1] - mo));
            const unsigned m0 = (crow(2 * i, 0) + 4 * hh <= r ? 0xFFFFu : 0u) | (crow(2 * i + 1, 0) + 4 * hh <= r ? 0xFFFF0000u : 0u);
            const unsigned m1 = (crow(8 + 2 * i, 0) + 4 * hh <= r ? 0xFFFFu : 0u) | (crow(8 + 2 * i + 1, 0) + 4 * hh <= r ? 0xFFFF0000u : 0u);
            d0[i] = p0[i] & m0; d1[i] = p1[i] & m1;
            p0[i] ^= d0[i]; p1[i] ^= d1[i];
        }
        o = MFMA(asbf(vf0[4]), asbf(d0), o); o = MFMA(asbf(vf1[4]), asbf(d1), o);
        if (i0 == 0) { o = MFMA(asbf(vf0[0]), asbf(p0), o); o = MFMA(asbf(vf1[0]), asbf(p1), o); }
    }
    WAIT_BAR(0);
    {
        const lptr s = lds + L_E + 4096 + (w ^ 1) * 3072 + lane * 16;
        const f32x4 a = *(LAS f32x4*)(s), c = *(LAS f32x4*)(s + 1024);
        const float lp = *(LAS float*)(lds + L_E + 4096 + (w ^ 1) * 3072 + 2048 + lane * 4);
        const float inv = __builtin_amdgcn_rcpf(o[8] + lp);
        u32x4 t;
        t[0] = nosent(pk((o[0] + a[0]) * inv, (o[1] + a[1]) * inv)); t[1] = nosent(pk((o[2] + a[2]) * inv, (o[3] + a[3]) * inv));
        t[2] = nosent(pk((o[4] + c[0]) * inv, (o[5] + c[1]) * inv)); t[3] = nosent(pk((o[6] + c[2]) * inv, (o[7] + c[3]) * inv));
        __builtin_amdgcn_raw_buffer_store_b128(t, rs, offO, 0, 16);
    }
    STAMP(8);
    const int qb3 = 2 * b + (g >> 2), nt = w & 3;
    bf16x8 wf[16];
    {
        const lptr base = lds + (4 * hh + q4) * 256 + 64 * (nt ^ q4) + 32 * blk + 8 * p4;
#pragma unroll
        for (int t = 0; t < 16; ++t) {
            const unsigned so0 = L_W + (t >> 2) * SLOT;
            const unsigned so1 = (t >> 2) < 2 ? L_W + (4 + (t >> 2)) * SLOT : L_X + ((t >> 2) - 2) * SLOT;
            wf[t] = trd2(base + (kh ? so1 : so0) + (t & 3) * 4096, 2048);
        }
    }
    u32x4 of[4];
    const unsigned rO = WS_O + (unsigned)(qb3 * 32 + 4 * w) * 1024 + lane * 16;
#pragma unroll
    for (int i = 0; i < 4; ++i) of[i] = __builtin_amdgcn_raw_buffer_load_b128(rs, rO + i * 1024, 0, 16);
    BAR();
    {
        int spins = 0;
        while (__any(umax2(umax2(umax4(of[0]), umax4(of[1])), umax2(umax4(of[2]), umax4(of[3]))) == ~0u) && ++spins < MAXSPIN) {
            __builtin_amdgcn_s_sleep(1);
#pragma unroll
            for (int i = 0; i < 4; ++i) of[i] = __builtin_amdgcn_raw_buffer_load_b128(rs, rO + i * 1024, 0, 16);
        }
    }
#pragma unroll
    for (int i = 0; i < 4; ++i) *(LAS u32x4*)(lds + L_E + (4 * w + i) * 1024 + lane * 16) = of[i];
    BAR();
    STAMP(9);
    acc = f32x16{};
    {
        const lptr ob = lds + L_E + kh * 16384 + lane * 16;
#pragma unroll
        for (int t = 0; t < 16; ++t) { const bf16x8 a = *(LAS bf16x8*)(ob + t * 1024); acc = MFMA(wf[t], a, acc); }
    }
    {
        const lptr d = lds + L_W + w * 2048 + lane * 16;
        *(LAS f32x4*)(d) = f32x4{acc[8], acc[9], acc[10], acc[11]};
        *(LAS f32x4*)(d + 1024) = f32x4{acc[12], acc[13], acc[14], acc[15]};
    }
    BAR();
    {
        const lptr s = lds + L_W + (w ^ 4) * 2048 + lane * 16;
        const f32x4 a = *(LAS f32x4*)(s), c = *(LAS f32x4*)(s + 1024);
        unsigned d0 = pk(acc[0] + a[0], acc[1] + a[1]), d1 = pk(acc[2] + a[2], acc[3] + a[3]);
        unsigned d2 = pk(acc[4] + c[0], acc[5] + c[1]), d3 = pk(acc[6] + c[2], acc[7] + c[3]);
        auto s0 = __builtin_amdgcn_permlane32_swap(d0, d2, false, false);
        auto s1 = __builtin_amdgcn_permlane32_swap(d1, d3, false, false);
        const u32x4 t = {s0[0], s1[0], s0[1], s1[1]};
        *(u32x4*)(out + (size_t)(32 * qb3 + r) * 1024 + (128 * cb + 32 * nt + 16 * kh + 8 * hh) * 2) = t;
    }
    if (tid == 0) {
        u64* pg = (u64*)(ws + WS_G + self * 128);
        const u64 val = hsh(gself, 31);
        asm volatile("global_store_dwordx2 %0, %1, off sc1" :: "v"(pg), "v"(val) : "memory");
    }
    STAMP(10);
}

extern "C" void kernel_launch(void* const* d_in, const int* in_sizes, int n_in,
                              void* d_out, int out_size, void* d_ws, size_t ws_size,
                              hipStream_t stream) {
    (void)hipFuncSetAttribute((const void*)fused_kernel, hipFuncAttributeMaxDynamicSharedMemorySize, LDS_BYTES);
    fused_kernel<<<256, 512, LDS_BYTES, stream>>>((const char*)d_in[0], (const char*)d_in[1], (const char*)d_in[2], (char*)d_out, (char*)d_ws);
}
```

```cpp
#include <hip/hip_runtime.h>
#include <hip/hip_bf16.h>
#include <stdint.h>

typedef __attribute__((ext_vector_type(8))) short bf16x8;
typedef __attribute__((ext_vector_type(4))) short s16x4;
typedef __attribute__((ext_vector_type(16))) float f32x16;
typedef __attribute__((ext_vector_type(4))) float f32x4;
typedef __attribute__((ext_vector_type(2))) float f32x2;
typedef __attribute__((ext_vector_type(4))) unsigned u32x4;
typedef __attribute__((ext_vector_type(2))) __bf16 bf16x2;
typedef unsigned long long u64;
#define LAS __attribute__((address_space(3)))
#define DI __device__ __forceinline__
typedef LAS char* lptr;

constexpr unsigned WS_K = 0, WS_V = 2u << 20, WS_O = 4u << 20, WS_G = 6u << 20, WS_F = WS_G + 256 * 128, WS_END = WS_F + 256 * 128;
constexpr unsigned SLOT = 16384;
constexpr unsigned L_X = 0;
constexpr unsigned L_W = 4 * SLOT;
constexpr unsigned L_R = 3 * SLOT;
constexpr unsigned L_M = L_W + 3 * SLOT;
constexpr unsigned L_E = 2 * SLOT;
constexpr unsigned LDS_BYTES = 10 * SLOT;
constexpr int MAXSPIN = 1 << 15;
constexpr float QSCALE = 0.25f * 1.4426950408889634f;

#define MFMA(a, b, c) __builtin_amdgcn_mfma_f32_32x32x16_bf16((a), (b), (c), 0, 0, 0)
#define WAIT_BAR(N) asm volatile("s_waitcnt vmcnt(" #N ") lgkmcnt(0)\n\ts_barrier" ::: "memory")
#define BAR() asm volatile("s_waitcnt lgkmcnt(0)\n\ts_barrier" ::: "memory")
#define MX3(a, b, c) __builtin_fmaxf(__builtin_fmaxf((a), (b)), (c))


DI unsigned pk(float a, float b) { f32x2 v = {a, b}; bf16x2 r = __builtin_convertvector(v, bf16x2); return __builtin_bit_cast(unsigned, r); }
DI unsigned nosent(unsigned x) { return x < 0xFFFFFFFEu ? x : 0xFFFFFFFEu; }
DI void glds16(unsigned voff, const char* sbase, unsigned lds_dst) {
    unsigned sv;
    asm volatile("s_mov_b32 %0, m0\n\ts_mov_b32 m0, %3\n\ts_nop 0\n\tglobal_load_lds_dwordx4 %1, %2\n\ts_mov_b32 m0, %0"
                 : "=&s"(sv) : "v"(voff), "s"(sbase), "s"(lds_dst) : "memory");
}
DI s16x4 trd(lptr p) { return __builtin_amdgcn_ds_read_tr16_b64_v4i16((LAS s16x4*)p); }
DI bf16x8 trd2(lptr p, int off2) { s16x4 a = trd(p), b = trd(p + off2); return __builtin_shufflevector(a, b, 0, 1, 2, 3, 4, 5, 6, 7); }
DI u64 hsh(u64 g, unsigned slot) { return g * 0x9E3779B97F4A7C15ull + (u64)(2u * slot + 1u) * 0xD1B54A32D192ED03ull; }
DI bf16x8 asbf(u32x4 v) { return __builtin_bit_cast(bf16x8, v); }
DI unsigned umax4(u32x4 v) { unsigned a = v.x > v.y ? v.x : v.y, b = v.z > v.w ? v.z : v.w; return a > b ? a : b; }
DI unsigned umax2(unsigned a, unsigned b) { return a > b ? a : b; }
DI float max16(const f32x16& s, float m) {
#pragma unroll
    for (int i = 0; i < 16; i += 2) m = MX3(m, s[i], s[i + 1]);
    return m;
}
DI float hmerge(float m) { auto rr = __builtin_amdgcn_permlane32_swap(__float_as_uint(m), __float_as_uint(m), false, false);
    return __builtin_fmaxf(__uint_as_float(rr[0]), __uint_as_float(rr[1])); }
constexpr int crow(int r, int hi) { return (r & 3) + 8 * (r >> 2) + 4 * hi; }
constexpr u64 diag_mask(int reg) { u64 m = 0; for (int l = 0; l < 64; ++l) if (crow(reg, l >> 5) <= (l & 31)) m |= 1ull << l; return m; }

DI void pv_tile(const f32x16& s, const u32x4& v0, const u32x4& v1, f32x16& o) {
    u32x4 p0, p1;
#pragma unroll
    for (int i = 0; i < 4; ++i) {
        p0[i] = pk(__builtin_amdgcn_exp2f(s[2 * i]), __builtin_amdgcn_exp2f(s[2 * i + 1]));
    }
    o = MFMA(asbf(v0), asbf(p0), o);
#pragma unroll
    for (int i = 0; i < 4; ++i) {
        p1[i] = pk(__builtin_amdgcn_exp2f(s[8 + 2 * i]), __builtin_amdgcn_exp2f(s[8 + 2 * i + 1]));
    }
    o = MFMA(asbf(v1), asbf(p1), o);
}

__global__ void __launch_bounds__(512, 2)
fused_kernel(const char* __restrict__ x, const char* __restrict__ wqkv, const char* __restrict__ wo, char* __restrict__ out, char* ws) {
    extern __shared__ __attribute__((aligned(16))) char smem[];
    const lptr lds = (lptr)smem;
    const unsigned lds0 = (unsigned)(uintptr_t)smem;
    const int tid = threadIdx.x, lane = tid & 63;
    const int w = __builtin_amdgcn_readfirstlane(tid >> 6);
    const int r = lane & 31, hh = lane >> 5;
    const int id = blockIdx.x, xcd = id & 7, idx = id >> 3;
    const int b = 8 * (xcd >> 1) + (idx >> 2), g = 4 * (xcd & 1) + (idx & 3);
    const int mt = w & 1, j = (w >> 1) & 1, kh = w >> 2;
    const int head = 4 * g + 2 * j + kh;
    const int qo = 2 * b + mt;
    const int self = b * 8 + g;
    const __amdgpu_buffer_rsrc_t rs = __builtin_amdgcn_make_buffer_rsrc(ws, 0, WS_END, 0x00020000);

    const int cons = lane < 4 ? (b + 1 + lane) * 8 + g : b * 8 + ((lane - 4) & 7);
    const bool cons_ok = lane < 4 ? (b + 1 + lane < 32) : (lane < 12);
    const unsigned cslot = lane < 4 ? lane : 4 + g;
    u64 gcons, gself;
    {
        const char* pc = ws + WS_G + (cons_ok ? cons : self) * 128;
        const char* ps = ws + WS_G + self * 128;
        asm volatile("global_load_dwordx2 %0, %1, off sc1" : "=v"(gcons) : "v"(pc) : "memory");
        asm volatile("global_load_dwordx2 %0, %1, off sc1" : "=v"(gself) : "v"(ps) : "memory");
    }
    const unsigned offK = WS_K + (unsigned)(head * 64 + qo) * 1024 + lane * 16;
    const unsigned offV = WS_V + (unsigned)(head * 64 + qo) * 1024 + (hh * 16 + (r & 15)) * 16;
    const unsigned offO = WS_O + (unsigned)(qo * 32 + head) * 1024 + lane * 16;
    {
        const u32x4 sent = {~0u, ~0u, ~0u, ~0u};
        __builtin_amdgcn_raw_buffer_store_b128(sent, rs, offK, 0, 16);
        if (r < 16) {
            __builtin_amdgcn_raw_buffer_store_b128(sent, rs, offV, 0, 16);
            __builtin_amdgcn_raw_buffer_store_b128(sent, rs, offV + 512, 0, 16);
        }
        __builtin_amdgcn_raw_buffer_store_b128(sent, rs, offO, 0, 16);
    }
    {
        const char* xb = x + (size_t)(64 * b + 8 * w) * 1024;
#pragma unroll
        for (int i = 0; i < 8; ++i) {
            const unsigned row15 = (unsigned)((8 * w + i) & 15);
            glds16(((unsigned)lane ^ row15) * 16u, xb + i * 1024, lds0 + L_X + (unsigned)(8 * w + i) * 1024);
        }
    }
    const unsigned voffW = (unsigned)(lane >> 3) * 3072u + (unsigned)((lane & 7) ^ (((lane >> 4) & 1) << 2)) * 16u;
    const char* wb = wqkv + (size_t)(16 * w) * 3072 + g * 128;
#define DMA_W(c, slotoff) do { \
        const char* s_ = wb + (size_t)(128 * ((c) & 3)) * 3072 + ((c) < 4 ? 1024 : ((c) < 8 ? 2048 : 0)); \
        glds16(voffW, s_, lds0 + (slotoff) + (unsigned)w * 2048); \
        glds16(voffW, s_ + 8 * 3072, lds0 + (slotoff) + (unsigned)w * 2048 + 1024); } while (0)
    DMA_W(0, L_W + 0 * SLOT); DMA_W(1, L_W + 1 * SLOT); DMA_W(2, L_W + 2 * SLOT);
    DMA_W(3, L_W + 3 * SLOT); DMA_W(4, L_W + 4 * SLOT); DMA_W(5, L_W + 5 * SLOT);
    asm volatile("s_waitcnt vmcnt(20)\n\ts_barrier" : "+v"(gcons), "+v"(gself) :: "memory");
    if (w == 0 && cons_ok) {
        u64* pf = (u64*)(ws + WS_F + cons * 128 + cslot * 8);
        const u64 val = hsh(gcons, cslot);
        asm volatile("global_store_dwordx2 %0, %1, off sc1" :: "v"(pf), "v"(val) : "memory");
    }
    WAIT_BAR(12);
    bf16x8 xf[16];
    {
        const int row = 32 * mt + r;
        const lptr xr = lds + L_X + row * 1024;
#pragma unroll
        for (int t = 0; t < 16; ++t) {
            const int ch = (4 * (t & 3) + 2 * kh + hh) ^ (row & 15);
            xf[t] = *(LAS bf16x8*)(xr + (t >> 2) * 256 + ch * 16);
        }
    }
    BAR();
    DMA_W(6, L_X + 0 * SLOT); DMA_W(7, L_X + 1 * SLOT); DMA_W(8, L_X + 2 * SLOT);

    const int blk = ((lane >> 4) & 1) ^ kh, q4 = (lane & 15) >> 2, p4 = lane & 3;
    const lptr wr = lds + (8 * hh + q4) * 128 + 64 * (j ^ (q4 >> 1)) + 32 * blk + 8 * p4 + kh * 2048;
    f32x16 acc;
#define STEP(slotoff, kc, SWAP) do { \
        bf16x8 wf_[4]; \
        _Pragma("unroll") for (int u = 0; u < 4; ++u) wf_[u] = trd2(wr + (slotoff) + u * 4096, 512); \
        _Pragma("unroll") for (int u = 0; u < 4; ++u) acc = SWAP ? MFMA(xf[4 * (kc) + u], wf_[u], acc) : MFMA(wf_[u], xf[4 * (kc) + u], acc); \
    } while (0)
    const lptr rw = lds + L_R + w * 2048 + lane * 16;
    const lptr rp = lds + L_R + (w ^ 4) * 2048 + lane * 16;

    u32x4 kf[5], vf0[5], vf1[5];
    acc = f32x16{};
    WAIT_BAR(16); STEP(L_W + 0 * SLOT, 0, false);
    WAIT_BAR(14); DMA_W(9, L_W + 0 * SLOT); STEP(L_W + 1 * SLOT, 1, false);
    WAIT_BAR(14); DMA_W(10, L_W + 1 * SLOT); STEP(L_W + 2 * SLOT, 2, false);
    WAIT_BAR(14); DMA_W(11, L_W + 2 * SLOT); STEP(L_W + 3 * SLOT, 3, false);
    *(LAS f32x4*)(rw) = f32x4{acc[8], acc[9], acc[10], acc[11]};
    *(LAS f32x4*)(rw + 1024) = f32x4{acc[12], acc[13], acc[14], acc[15]};
    BAR();
    {
        const f32x4 a = *(LAS f32x4*)(rp), c = *(LAS f32x4*)(rp + 1024);
        u32x4 k4;
        k4[0] = pk(acc[0] + a[0], acc[1] + a[1]); k4[1] = pk(acc[2] + a[2], acc[3] + a[3]);
        k4[2] = pk(acc[4] + c[0], acc[5] + c[1]); k4[3] = pk(acc[6] + c[2], acc[7] + c[3]);
#pragma unroll
        for (int i = 0; i < 4; ++i) k4[i] = nosent(k4[i]);
        kf[4] = k4;
        __builtin_amdgcn_raw_buffer_store_b128(k4, rs, offK, 0, 16);
    }
    acc = f32x16{};
    WAIT_BAR(15); STEP(L_W + 4 * SLOT, 0, true);
    WAIT_BAR(13); STEP(L_W + 5 * SLOT, 1, true);
    WAIT_BAR(11); STEP(L_X + 0 * SLOT, 2, true);
    WAIT_BAR(9); STEP(L_X + 1 * SLOT, 3, true);
    {
        const int li = hh * 16 + (r & 15);
        if (r >= 16) {
            const lptr d = lds + L_R + w * 2048 + li * 16;
#pragma unroll
            for (int i = 0; i < 4; ++i) *(LAS f32x4*)(d + i * 512) = f32x4{acc[4 * i], acc[4 * i + 1], acc[4 * i + 2], acc[4 * i + 3]};
        }
        BAR();
        u32x4 a0 = {0x3F803F80u, 0x3F803F80u, 0x3F803F80u, 0x3F803F80u}, a1 = a0;
        if (r < 16) {
            const lptr s = lds + L_R + (w ^ 4) * 2048 + li * 16;
            f32x4 pp[4];
#pragma unroll
            for (int i = 0; i < 4; ++i) pp[i] = *(LAS f32x4*)(s + i * 512);
#pragma unroll
            for (int i = 0; i < 2; ++i) {
                a0[2 * i] = nosent(pk(acc[4 * i] + pp[i][0], acc[4 * i + 1] + pp[i][1]));
                a0[2 * i + 1] = nosent(pk(acc[4 * i + 2] + pp[i][2], acc[4 * i + 3] + pp[i][3]));
                a1[2 * i] = nosent(pk(acc[8 + 4 * i] + pp[2 + i][0], acc[8 + 4 * i + 1] + pp[2 + i][1]));
                a1[2 * i + 1] = nosent(pk(acc[8 + 4 * i + 2] + pp[2 + i][2], acc[8 + 4 * i + 3] + pp[2 + i][3]));
            }
            __builtin_amdgcn_raw_buffer_store_b128(a0, rs, offV, 0, 16);
            __builtin_amdgcn_raw_buffer_store_b128(a1, rs, offV + 512, 0, 16);
        }
        vf0[4] = a0; vf1[4] = a1;
    }
    u64 fw;
    const char* pfs = ws + WS_F + self * 128 + (lane < 12 ? lane : 0) * 8;
    acc = f32x16{};
    WAIT_BAR(9);
    asm volatile("global_load_dwordx2 %0, %1, off sc1" : "=v"(fw) : "v"(pfs) : "memory");
    STEP(L_X + 2 * SLOT, 0, false);
    WAIT_BAR(8); STEP(L_W + 0 * SLOT, 1, false);
    WAIT_BAR(6); STEP(L_W + 1 * SLOT, 2, false);
    asm volatile("s_waitcnt vmcnt(0) lgkmcnt(0)\n\ts_barrier" : "+v"(fw) :: "memory");
    const int i0 = b < 4 ? 4 - b : 0;
    {
        const bool need = lane < 4 ? (b - 1 - lane >= 0) : (lane < 12);
        const u64 want = hsh(gself, lane < 12 ? lane : 0);
        int spins = 0;
        while (__any(need && fw != want) && ++spins < MAXSPIN) {
            __builtin_amdgcn_s_sleep(2);
            asm volatile("global_load_dwordx2 %0, %1, off sc1\n\ts_waitcnt vmcnt(0)" : "=v"(fw) : "v"(pfs) : "memory");
        }
    }
    const unsigned rK = offK - 8 * 1024, rV = offV - 8 * 1024;
#pragma unroll
    for (int i = 0; i < 4; ++i) {
        kf[i] = u32x4{0, 0, 0, 0};
        if (i >= i0) kf[i] = __builtin_amdgcn_raw_buffer_load_b128(rs, rK + i * 2048, 0, 16);
    }
    STEP(L_W + 2 * SLOT, 3, false);
    {
        const lptr mw = lds + L_M + w * 2048 + lane * 16;
        *(LAS f32x4*)(rw) = f32x4{acc[0], acc[1], acc[2], acc[3]};
        *(LAS f32x4*)(rw + 1024) = f32x4{acc[4], acc[5], acc[6], acc[7]};
        *(LAS f32x4*)(mw) = f32x4{acc[8], acc[9], acc[10], acc[11]};
        *(LAS f32x4*)(mw + 1024) = f32x4{acc[12], acc[13], acc[14], acc[15]};
    }
    BAR();
    bf16x8 qfo, qfx;
    {
        const lptr m4 = lds + L_M + (w ^ 4) * 2048 + lane * 16;
        const lptr r1 = lds + L_R + (w ^ 1) * 2048 + lane * 16;
        const lptr m5 = lds + L_M + (w ^ 5) * 2048 + lane * 16;
        const f32x4 a = *(LAS f32x4*)(m4), c = *(LAS f32x4*)(m4 + 1024);
        const f32x4 d0 = *(LAS f32x4*)(r1), d1 = *(LAS f32x4*)(r1 + 1024);
        const f32x4 e0 = *(LAS f32x4*)(m5), e1 = *(LAS f32x4*)(m5 + 1024);
        u32x4 t;
        t[0] = pk((acc[0] + a[0]) * QSCALE, (acc[1] + a[1]) * QSCALE); t[1] = pk((acc[2] + a[2]) * QSCALE, (acc[3] + a[3]) * QSCALE);
        t[2] = pk((acc[4] + c[0]) * QSCALE, (acc[5] + c[1]) * QSCALE); t[3] = pk((acc[6] + c[2]) * QSCALE, (acc[7] + c[3]) * QSCALE);
        qfo = asbf(t);
        t[0] = pk((d0[0] + e0[0]) * QSCALE, (d0[1] + e0[1]) * QSCALE); t[1] = pk((d0[2] + e0[2]) * QSCALE, (d0[3] + e0[3]) * QSCALE);
        t[2] = pk((d1[0] + e1[0]) * QSCALE, (d1[1] + e1[1]) * QSCALE); t[3] = pk((d1[2] + e1[2]) * QSCALE, (d1[3] + e1[3]) * QSCALE);
        qfx = asbf(t);
    }
#pragma unroll
    for (int i = 0; i < 4; ++i) {
        vf0[i] = u32x4{0x3F803F80u, 0x3F803F80u, 0x3F803F80u, 0x3F803F80u}; vf1[i] = vf0[i];
        if (i >= i0 && r < 16) {
            vf0[i] = __builtin_amdgcn_raw_buffer_load_b128(rs, rV + i * 2048, 0, 16);
            vf1[i] = __builtin_amdgcn_raw_buffer_load_b128(rs, rV + i * 2048 + 512, 0, 16);
        }
    }
    const f32x16 zero = {};
    const float NINF = -__builtin_inff();
    float mo = NINF, mx = NINF;
    f32x16 sc = MFMA(asbf(kf[4]), qfo, zero);
    if (mt == 0) { const f32x16 s = MFMA(asbf(kf[4]), qfx, zero); mx = max16(s, mx); }
    {
        int spins = 0;
        while (__any(umax2(umax2(umax4(kf[0]), umax4(kf[1])), umax2(umax4(kf[2]), umax4(kf[3]))) == ~0u) && ++spins < MAXSPIN) {
            __builtin_amdgcn_s_sleep(1);
#pragma unroll
            for (int i = 0; i < 4; ++i) if (i >= i0) kf[i] = __builtin_amdgcn_raw_buffer_load_b128(rs, rK + i * 2048, 0, 16);
        }
    }
    {
        f32x16 sf;
        if (i0 == 0) sf = MFMA(asbf(kf[0]), qfo, zero);
        else {
#pragma unroll
            for (int i = 0; i < 16; ++i) sf[i] = NINF;
        }
#pragma unroll
        for (int i = 0; i < 16; ++i) sc[i] = __builtin_amdgcn_inverse_ballot_w64(diag_mask(i)) ? sc[i] : sf[i];
        mo = max16(sc, mo);
    }
    if (mt == 1 && i0 == 0) { const f32x16 s = MFMA(asbf(kf[0]), qfx, zero); mx = max16(s, mx); }
#pragma unroll
    for (int i = 1; i < 4; ++i) {
        if (i >= i0) {
            const f32x16 s1 = MFMA(asbf(kf[i]), qfo, zero), s2 = MFMA(asbf(kf[i]), qfx, zero);
            mo = max16(s1, mo); mx = max16(s2, mx);
        }
    }
    mo = hmerge(mo); mx = hmerge(mx);
    *(LAS f32x2*)(lds + L_E + w * 512 + lane * 8) = f32x2{mo, mx};
    {
        int spins = 0;
        for (;;) {
            unsigned m = 0;
#pragma unroll
            for (int i = 0; i < 4; ++i) m = umax2(m, umax2(umax4(vf0[i]), umax4(vf1[i])));
            if (!__any(m == ~0u) || ++spins >= MAXSPIN) break;
            __builtin_amdgcn_s_sleep(1);
#pragma unroll
            for (int i = 0; i < 4; ++i) if (i >= i0 && r < 16) {
                vf0[i] = __builtin_amdgcn_raw_buffer_load_b128(rs, rV + i * 2048, 0, 16);
                vf1[i] = __builtin_amdgcn_raw_buffer_load_b128(rs, rV + i * 2048 + 512, 0, 16);
            }
        }
    }
    BAR();
    const int cb = g & 3;
    {
        const unsigned voffO = (unsigned)(lane >> 4) * 1024u + (unsigned)((lane & 15) ^ (((lane >> 4) & 3) << 2)) * 16u;
        const char* ob = wo + (size_t)(8 * w) * 1024 + cb * 256;
#pragma unroll
        for (int c = 0; c < 8; ++c) {
            const unsigned so = (c < 6 ? L_W + c * SLOT : L_X + (c - 6) * SLOT) + (unsigned)w * 2048;
            glds16(voffO, ob + (size_t)(64 * c) * 1024, lds0 + so);
            glds16(voffO, ob + (size_t)(64 * c + 4) * 1024, lds0 + so + 1024);
        }
    }
    {
        const f32x2 pm = *(LAS f32x2*)(lds + L_E + (w ^ 1) * 512 + lane * 8);
        mo = __builtin_fmaxf(mo, pm[1]); mx = __builtin_fmaxf(mx, pm[0]);
    }
    {
        f32x16 nm;
#pragma unroll
        for (int i = 0; i < 16; ++i) nm[i] = -mx;
        f32x16 o = {};
        if (mt == 0) { const f32x16 s = MFMA(asbf(kf[4]), qfx, nm); pv_tile(s, vf0[4], vf1[4], o); }
        else if (i0 == 0) { const f32x16 s = MFMA(asbf(kf[0]), qfx, nm); pv_tile(s, vf0[0], vf1[0], o); }
#pragma unroll
        for (int i = 1; i < 4; ++i) if (i >= i0) { const f32x16 s = MFMA(asbf(kf[i]), qfx, nm); pv_tile(s, vf0[i], vf1[i], o); }
        const lptr d = lds + L_E + 4096 + w * 3072 + lane * 16;
        *(LAS f32x4*)(d) = f32x4{o[0], o[1], o[2], o[3]};
        *(LAS f32x4*)(d + 1024) = f32x4{o[4], o[5], o[6], o[7]};
        *(LAS float*)(lds + L_E + 4096 + w * 3072 + 2048 + lane * 4) = o[8];
    }
    f32x16 o = {};
    {
        f32x16 nm;
#pragma unroll
        for (int i = 0; i < 16; ++i) nm[i] = -mo;
#pragma unroll
        for (int i = 1; i < 4; ++i) if (i >= i0) { const f32x16 s = MFMA(asbf(kf[i]), qfo, nm); pv_tile(s, vf0[i], vf1[i], o); }
        u32x4 p0, p1, d0, d1;
#pragma unroll
        for (int i = 0; i < 4; ++i) {
            p0[i] = pk(__builtin_amdgcn_exp2f(sc[2 * i] - mo), __builtin_amdgcn_exp2f(sc[2 * i + 1] - mo));
            p1[i] = pk(__builtin_amdgcn_exp2f(sc[8 + 2 * i] - mo), __builtin_amdgcn_exp2f(sc[8 + 2 * i + 1] - mo));
            const unsigned m0 = (crow(2 * i, 0) + 4 * hh <= r ? 0xFFFFu : 0u) | (crow(2 * i + 1, 0) + 4 * hh <= r ? 0xFFFF0000u : 0u);
            const unsigned m1 = (crow(8 + 2 * i, 0) + 4 * hh <= r ? 0xFFFFu : 0u) | (crow(8 + 2 * i + 1, 0) + 4 * hh <= r ? 0xFFFF0000u : 0u);
            d0[i] = p0[i] & m0; d1[i] = p1[i] & m1;
            p0[i] ^= d0[i]; p1[i] ^= d1[i];
        }
        o = MFMA(asbf(vf0[4]), asbf(d0), o); o = MFMA(asbf(vf1[4]), asbf(d1), o);
        if (i0 == 0) { o = MFMA(asbf(vf0[0]), asbf(p0), o); o = MFMA(asbf(vf1[0]), asbf(p1), o); }
    }
    WAIT_BAR(0);
    {
        const lptr s = lds + L_E + 4096 + (w ^ 1) * 3072 + lane * 16;
        const f32x4 a = *(LAS f32x4*)(s), c = *(LAS f32x4*)(s + 1024);
        const float lp = *(LAS float*)(lds + L_E + 4096 + (w ^ 1) * 3072 + 2048 + lane * 4);
        const float inv = __builtin_amdgcn_rcpf(o[8] + lp);
        u32x4 t;
        t[0] = nosent(pk((o[0] + a[0]) * inv, (o[1] + a[1]) * inv)); t[1] = nosent(pk((o[2] + a[2]) * inv, (o[3] + a[3]) * inv));
        t[2] = nosent(pk((o[4] + c[0]) * inv, (o[5] + c[1]) * inv)); t[3] = nosent(pk((o[6] + c[2]) * inv, (o[7] + c[3]) * inv));
        __builtin_amdgcn_raw_buffer_store_b128(t, rs, offO, 0, 16);
    }
    const int qb3 = 2 * b + (g >> 2), nt = w & 3;
    bf16x8 wf[16];
    {
        const lptr base = lds + (4 * hh + q4) * 256 + 64 * (nt ^ q4) + 32 * blk + 8 * p4;
#pragma unroll
        for (int t = 0; t < 16; ++t) {
            const unsigned so0 = L_W + (t >> 2) * SLOT;
            const unsigned so1 = (t >> 2) < 2 ? L_W + (4 + (t >> 2)) * SLOT : L_X + ((t >> 2) - 2) * SLOT;
            wf[t] = trd2(base + (kh ? so1 : so0) + (t & 3) * 4096, 2048);
        }
    }
    u32x4 of[4];
    const unsigned rO = WS_O + (unsigned)(qb3 * 32 + 4 * w) * 1024 + lane * 16;
#pragma unroll
    for (int i = 0; i < 4; ++i) of[i] = __builtin_amdgcn_raw_buffer_load_b128(rs, rO + i * 1024, 0, 16);
    BAR();
    {
        int spins = 0;
        while (__any(umax2(umax2(umax4(of[0]), umax4(of[1])), umax2(umax4(of[2]), umax4(of[3]))) == ~0u) && ++spins < MAXSPIN) {
            __builtin_amdgcn_s_sleep(1);
#pragma unroll
            for (int i = 0; i < 4; ++i) of[i] = __builtin_amdgcn_raw_buffer_load_b128(rs, rO + i * 1024, 0, 16);
        }
    }
#pragma unroll
    for (int i = 0; i < 4; ++i) *(LAS u32x4*)(lds + L_E + (4 * w + i) * 1024 + lane * 16) = of[i];
    BAR();
    acc = f32x16{};
    {
        const lptr ob = lds + L_E + kh * 16384 + lane * 16;
#pragma unroll
        for (int t = 0; t < 16; ++t) { const bf16x8 a = *(LAS bf16x8*)(ob + t * 1024); acc = MFMA(wf[t], a, acc); }
    }
    {
        const lptr d = lds + L_W + w * 2048 + lane * 16;
        *(LAS f32x4*)(d) = f32x4{acc[8], acc[9], acc[10], acc[11]};
        *(LAS f32x4*)(d + 1024) = f32x4{acc[12], acc[13], acc[14], acc[15]};
    }
    BAR();
    {
        const lptr s = lds + L_W + (w ^ 4) * 2048 + lane * 16;
        const f32x4 a = *(LAS f32x4*)(s), c = *(LAS f32x4*)(s + 1024);
        unsigned d0 = pk(acc[0] + a[0], acc[1] + a[1]), d1 = pk(acc[2] + a[2], acc[3] + a[3]);
        unsigned d2 = pk(acc[4] + c[0], acc[5] + c[1]), d3 = pk(acc[6] + c[2], acc[7] + c[3]);
        auto s0 = __builtin_amdgcn_permlane32_swap(d0, d2, false, false);
        auto s1 = __builtin_amdgcn_permlane32_swap(d1, d3, false, false);
        const u32x4 t = {s0[0], s1[0], s0[1], s1[1]};
        *(u32x4*)(out + (size_t)(32 * qb3 + r) * 1024 + (128 * cb + 32 * nt + 16 * kh + 8 * hh) * 2) = t;
    }
    if (tid == 0) {
        u64* pg = (u64*)(ws + WS_G + self * 128);
        const u64 val = hsh(gself, 31);
        asm volatile("global_store_dwordx2 %0, %1, off sc1" :: "v"(pg), "v"(val) : "memory");
    }
}

extern "C" void kernel_launch(void* const* d_in, const int* in_sizes, int n_in,
                              void* d_out, int out_size, void* d_ws, size_t ws_size,
                              hipStream_t stream) {
    (void)hipFuncSetAttribute((const void*)fused_kernel, hipFuncAttributeMaxDynamicSharedMemorySize, LDS_BYTES);
    fused_kernel<<<256, 512, LDS_BYTES, stream>>>((const char*)d_in[0], (const char*)d_in[1], (const char*)d_in[2], (char*)d_out, (char*)d_ws);
}
```

```cpp
#include <hip/hip_runtime.h>
#include <hip/hip_bf16.h>
#include <stdint.h>

typedef __attribute__((ext_vector_type(8))) short bf16x8;
typedef __attribute__((ext_vector_type(4))) short s16x4;
typedef __attribute__((ext_vector_type(16))) float f32x16;
typedef __attribute__((ext_vector_type(4))) float f32x4;
typedef __attribute__((ext_vector_type(2))) float f32x2;
typedef __attribute__((ext_vector_type(4))) unsigned u32x4;
typedef __attribute__((ext_vector_type(2))) unsigned u32x2;
typedef __attribute__((ext_vector_type(2))) __bf16 bf16x2;
typedef unsigned long long u64;
#define LAS __attribute__((address_space(3)))
#define DI __device__ __forceinline__
typedef LAS char* lptr;

constexpr unsigned REC = 1280;
constexpr unsigned WS_K = 0, WS_V = 3u << 20, WS_O = 6u << 20, WS_G = 9u << 20, WS_F = WS_G + 256 * 128, WS_END = WS_F + 256 * 128;
constexpr unsigned WS_OOB = 0x7FFFF000u;
constexpr unsigned HS = 8192;
constexpr unsigned L_R = 6 * HS;
constexpr unsigned L_M = 14 * HS;
constexpr unsigned L_E = 16 * HS;
constexpr unsigned LDS_BYTES = 20 * HS;
constexpr int MAXSPIN = 1 << 15;
constexpr float QSCALE = 0.25f * 1.4426950408889634f;
constexpr unsigned ONES = 0x3F803F80u;

#define MFMA(a, b, c) __builtin_amdgcn_mfma_f32_32x32x16_bf16((a), (b), (c), 0, 0, 0)
#define MFMA16(a, b, c) __builtin_amdgcn_mfma_f32_16x16x32_bf16((a), (b), (c), 0, 0, 0)
#define SB() __builtin_amdgcn_sched_barrier(0)
#define PIN(x) asm volatile("" : "+v"(x))
#define PINS(x) asm volatile("" : "+s"(x))
#define FENCE() asm volatile("" ::: "memory")
constexpr int wcnt(int vm, int lgkm) { return (vm & 15) | (7 << 4) | (lgkm << 8) | ((vm >> 4) << 14); }
#define WAIT_BAR(N) do { FENCE(); __builtin_amdgcn_s_waitcnt(wcnt(N, 0)); __builtin_amdgcn_s_barrier(); FENCE(); SB(); } while (0)
#define BAR() WAIT_BAR(63)
#define WAIT_BAR2(N0, N1) do { FENCE(); if (kh) __builtin_amdgcn_s_waitcnt(wcnt(N1, 0)); else __builtin_amdgcn_s_waitcnt(wcnt(N0, 0)); __builtin_amdgcn_s_barrier(); FENCE(); SB(); } while (0)
#define MX3(a, b, c) __builtin_fmaxf(__builtin_fmaxf((a), (b)), (c))
#define ST_SC1(v, off) __builtin_amdgcn_raw_buffer_store_b128((v), rs, (off), 0, 16)
#define ST1_SC1(v, off) __builtin_amdgcn_raw_buffer_store_b32((v), rs, (off), 0, 16)
#define ALD4(dst, off, imm) asm volatile("buffer_load_dwordx4 %0, %1, %2, 0 offen offset:" #imm " sc1" : "=v"(dst) : "v"(off), "s"(rs) : "memory")
#define ALD1(dst, off, imm) asm volatile("buffer_load_dword %0, %1, %2, 0 offen offset:" #imm " sc1" : "=v"(dst) : "v"(off), "s"(rs) : "memory")


DI unsigned pk(float a, float b) { f32x2 v = {a, b}; bf16x2 r = __builtin_convertvector(v, bf16x2); return __builtin_bit_cast(unsigned, r); }
DI void glds16(unsigned voff, const char* sbase, unsigned lds_dst) {
    asm volatile("s_mov_b32 m0, %2\n\ts_nop 0\n\tglobal_load_lds_dwordx4 %0, %1" :: "v"(voff), "s"(sbase), "s"(lds_dst) : "memory");
}
DI s16x4 trd(lptr p) { return __builtin_amdgcn_ds_read_tr16_b64_v4i16((LAS s16x4*)p); }
DI bf16x8 trd2(lptr p, int off2) { s16x4 a = trd(p), b = trd(p + off2); return __builtin_shufflevector(a, b, 0, 1, 2, 3, 4, 5, 6, 7); }
DI u64 hsh(u64 g, unsigned slot) { return g * 0x9E3779B97F4A7C15ull + (u64)(2u * slot + 1u) * 0xD1B54A32D192ED03ull; }
DI bf16x8 asbf(u32x4 v) { return __builtin_bit_cast(bf16x8, v); }
typedef __attribute__((ext_vector_type(2))) unsigned short u16x2;
DI unsigned d2(unsigned a, unsigned k, unsigned c) { return __builtin_amdgcn_udot2(__builtin_bit_cast(u16x2, a), __builtin_bit_cast(u16x2, k), c, false); }
DI unsigned csum(u32x4 v, unsigned c) { return d2(v.x, 0x682f978bu, d2(v.y, 0x92e1a2d1u, d2(v.z, 0x4ca14349u, d2(v.w, 0xf6314f97u, c)))); }
DI float max16(const f32x16& s, float m) {
#pragma unroll
    for (int i = 0; i < 16; i += 2) m = MX3(m, s[i], s[i + 1]);
    return m;
}
DI float hmerge(float m) { auto rr = __builtin_amdgcn_permlane32_swap(__float_as_uint(m), __float_as_uint(m), false, false);
    return __builtin_fmaxf(__uint_as_float(rr[0]), __uint_as_float(rr[1])); }
constexpr int crow(int r, int hi) { return (r & 3) + 8 * (r >> 2) + 4 * hi; }
constexpr u64 diag_mask(int reg) { u64 m = 0; for (int l = 0; l < 64; ++l) if (crow(reg, l >> 5) <= (l & 31)) m |= 1ull << l; return m; }

DI void exp_tile(const f32x16& s, u32x4& p0, u32x4& p1) {
#pragma unroll
    for (int i = 0; i < 4; ++i) p0[i] = pk(__builtin_amdgcn_exp2f(s[2 * i]), __builtin_amdgcn_exp2f(s[2 * i + 1]));
#pragma unroll
    for (int i = 0; i < 4; ++i) p1[i] = pk(__builtin_amdgcn_exp2f(s[8 + 2 * i]), __builtin_amdgcn_exp2f(s[8 + 2 * i + 1]));
}
#define PV(o, va, vb, p0, p1) do { o = MFMA(asbf(va), asbf(p0), o); o = MFMA(asbf(vb), asbf(p1), o); } while (0)

__global__ void __launch_bounds__(512, 2)
fused_kernel(const char* __restrict__ x, const char* __restrict__ wqkv, const char* __restrict__ wo, char* __restrict__ out, char* ws) {
    extern __shared__ __attribute__((aligned(16))) char smem[];
    const lptr lds = (lptr)smem;
    const unsigned lds0 = (unsigned)(uintptr_t)smem;
    const int tid = threadIdx.x, lane = tid & 63;
    const int w = __builtin_amdgcn_readfirstlane(tid >> 6);
    const int r = lane & 31, hh = lane >> 5;
    const int id = blockIdx.x, xcd = id & 7, idx = id >> 3;
    const int b = 8 * (xcd >> 1) + (idx >> 2), g = 4 * (xcd & 1) + (idx & 3);
    const int rho = ((idx >> 2) + 2 * (idx & 3)) & 7;
    const int mt = w & 1, hl = w >> 1, yg = w >> 2;
    const int head = 4 * g + hl;
    const int qo = 2 * b + mt;
    const int self = b * 8 + g;
    const int i0 = b < 4 ? 4 - b : 0;
    PINS(wo); PINS(out);
    const __amdgpu_buffer_rsrc_t rs = __builtin_amdgcn_make_buffer_rsrc(ws, 0, WS_END, 0x00020000);
    const int cons = lane < 4 ? (b + 1 + lane) * 8 + g : b * 8 + ((lane - 4) & 7);
    const bool cons_ok = lane < 4 ? (b + 1 + lane < 32) : (lane < 12);
    const unsigned cslot = lane < 4 ? lane : 4 + g;
    u64 gcons, gself;
    {
        const char* pc = ws + WS_G + (cons_ok ? cons : self) * 128;
        const char* ps = ws + WS_G + self * 128;
        asm volatile("global_load_dwordx2 %0, %1, off sc1" : "=v"(gcons) : "v"(pc) : "memory");
        asm volatile("global_load_dwordx2 %0, %1, off sc1" : "=v"(gself) : "v"(ps) : "memory");
    }
    const unsigned voffX = (unsigned)(lane >> 3) * 1024u + (unsigned)((lane & 7) ^ ((4 * w + (lane >> 4)) & 7)) * 16u;
    const char* xb = x + (size_t)(64 * b + 8 * w) * 1024;
    const unsigned voffW = (unsigned)(lane >> 3) * 3072u + (unsigned)((lane & 7) ^ ((((lane >> 4) & 1) | ((w & 1) << 1)) << 1)) * 16u;
    const char* wb = wqkv + (size_t)(8 * w) * 3072 + g * 128;
    const unsigned ldw = lds0 + (unsigned)w * 1024;
#define DMA_X(p) glds16(voffX, xb + (((p) + rho) & 7) * 128, ldw + (p) * HS)
#define DMA_W(matoff, p, hs) glds16(voffW, wb + (size_t)(((p) + rho) & 7) * (64 * 3072) + (matoff), ldw + (hs) * HS)
#define DMA_K(p) DMA_W(1024, p, 8 + (p))
#define DMA_V(p, hs) DMA_W(2048, p, hs)
#define DMA_Q(p, hs) DMA_W(0, p, hs)
    DMA_X(0); DMA_K(0); DMA_X(1); DMA_K(1); DMA_V(0, 16); DMA_Q(0, 17); DMA_X(2); DMA_K(2); DMA_X(3); DMA_K(3);
    const unsigned recK = WS_K + (unsigned)(head * 64 + qo) * REC, recV = WS_V + (unsigned)(head * 64 + qo) * REC;
    const int c16 = lane & 15, g4 = lane >> 4, q4 = c16 >> 2, p4 = lane & 3;
    lptr wr0 = lds + (8 * g4 + q4) * 128 + 32 * (hl ^ ((q4 >> 1) | ((g4 & 1) << 1))) + 8 * p4;
    lptr wr1 = wr0 + 8 * HS, wr2 = wr0 + 16 * HS;
    asm volatile("" : "+v"(wr1), "+v"(wr2));
    const int xrow = 32 * mt + c16;
    const lptr xr0 = lds + xrow * 128 + ((g4 ^ ((xrow >> 1) & 7)) * 16);
    const lptr xr1 = lds + xrow * 128 + (((4 + g4) ^ ((xrow >> 1) & 7)) * 16);
    u32x4 dm0, dm1;
#pragma unroll
    for (int i = 0; i < 4; ++i) {
        dm0[i] = (crow(2 * i, 0) + 4 * hh <= r ? 0xFFFFu : 0u) | (crow(2 * i + 1, 0) + 4 * hh <= r ? 0xFFFF0000u : 0u);
        dm1[i] = (crow(8 + 2 * i, 0) + 4 * hh <= r ? 0xFFFFu : 0u) | (crow(8 + 2 * i + 1, 0) + 4 * hh <= r ? 0xFFFF0000u : 0u);
    }
    PIN(dm0); PIN(dm1);
    bf16x8 xa[16], xc[16], wa[4], wbf[4];
    f32x4 ac0, ac1;
#define WRP(hs) (((hs) < 8 ? wr0 : ((hs) < 16 ? wr1 : wr2)) + ((hs) & 7) * HS)
#define RW(dst, u, hs0, hs1) dst[u] = trd2(WRP((u) < 2 ? (hs0) : (hs1)) + ((u) & 1) * 4096, 512)
#define RX(c, u) do { const lptr p_ = (((u) & 1) ? xr1 : xr0) + (2 * (c) + ((u) >> 1)) * HS; xa[4 * (c) + (u)] = *(LAS bf16x8*)(p_); xc[4 * (c) + (u)] = *(LAS bf16x8*)(p_ + 2048); } while (0)
#define M1(src, c, u, SWAP) do { ac0 = SWAP ? MFMA16(xa[4 * (c) + (u)], src[u], ac0) : MFMA16(src[u], xa[4 * (c) + (u)], ac0); \
                                 ac1 = SWAP ? MFMA16(xc[4 * (c) + (u)], src[u], ac1) : MFMA16(src[u], xc[4 * (c) + (u)], ac1); PIN(ac0); PIN(ac1); } while (0)
#define STEP(cur, c, SWAP, nxt, hs0, hs1, MEM) do { if (yg) { MEM; } SB(); \
        M1(cur, c, 0, SWAP); RW(nxt, 0, hs0, hs1); RW(nxt, 1, hs0, hs1); SB(); \
        M1(cur, c, 1, SWAP); RW(nxt, 2, hs0, hs1); RW(nxt, 3, hs0, hs1); SB(); \
        M1(cur, c, 2, SWAP); M1(cur, c, 3, SWAP); SB(); if (!yg) { MEM; } } while (0)
#define STEPX(cur, c, nxt, hs0, hs1, MEM) do { if (yg) { MEM; } SB(); \
        M1(cur, c, 0, false); RX((c) + 1, 0); RW(nxt, 0, hs0, hs1); RX((c) + 1, 1); SB(); \
        M1(cur, c, 1, false); RW(nxt, 1, hs0, hs1); RX((c) + 1, 2); RW(nxt, 2, hs0, hs1); SB(); \
        M1(cur, c, 2, false); RX((c) + 1, 3); RW(nxt, 3, hs0, hs1); SB(); \
        M1(cur, c, 3, false); SB(); if (!yg) { MEM; } } while (0)
#define FOLD(dst, SC) do { const unsigned a0_ = pk(ac0[0] * (SC), ac0[1] * (SC)), a1_ = pk(ac0[2] * (SC), ac0[3] * (SC)), b0_ = pk(ac1[0] * (SC), ac1[1] * (SC)), b1_ = pk(ac1[2] * (SC), ac1[3] * (SC)); \
        auto t0_ = __builtin_amdgcn_permlane16_swap(a0_, b0_, false, false); auto t1_ = __builtin_amdgcn_permlane16_swap(a1_, b1_, false, false); \
        dst[0] = t0_[0]; dst[1] = t1_[0]; dst[2] = t0_[1]; dst[3] = t1_[1]; } while (0)
    u32x4 k0, k1, k2, k3, k4, va0, va1, va2, va3, va4, vb0, vb1, vb2, vb3, vb4;
    unsigned ck0, ck1, ck2, ck3, cv0, cv1, cv2, cv3;
    const char* pfs = ws + WS_F + (unsigned)self * 128 + (lane < 12 ? lane : 0) * 8;

    ac0 = f32x4{}; ac1 = f32x4{};
    WAIT_BAR(6);
    asm volatile("" : "+v"(gcons), "+v"(gself));
    const unsigned nonce = __builtin_amdgcn_readfirstlane((unsigned)(hsh(gself, 77) >> 32));
#pragma unroll
    for (int u = 0; u < 4; ++u) { RX(0, u); RW(wa, u, 8, 9); }
    SB();
    DMA_X(4); DMA_K(4); DMA_X(5); DMA_K(5);
    WAIT_BAR(4);
    STEPX(wa, 0, wbf, 10, 11, DMA_X(6); DMA_K(6); DMA_X(7); DMA_K(7));
    WAIT_BAR(4);
    STEPX(wbf, 1, wa, 12, 13, DMA_V(1, 18); DMA_V(2, 19); DMA_V(3, 0));
    WAIT_BAR(3);
    STEPX(wa, 2, wbf, 14, 15, DMA_V(4, 1); DMA_V(5, 8));
    WAIT_BAR(4);
    STEP(wbf, 3, false, wa, 16, 18, DMA_V(6, 9); DMA_V(7, 2));
    FOLD(k4, 1.0f);
    ST_SC1(k4, recK + lane * 16);
    ST1_SC1(nonce - csum(k4, 0), recK + 1024 + lane * 4);
    if (w == 0 && cons_ok) {
        const u64 val = (hsh(gcons, cslot) & 0xFFFFFFFF00000000ull) | nonce;
        asm volatile("global_store_dwordx2 %0, %1, off sc1" :: "v"(ws + WS_F + cons * 128 + cslot * 8), "v"(val) : "memory");
    }
    ac0 = f32x4{}; ac1 = f32x4{};
    WAIT_BAR(6);
    STEP(wa, 0, true, wbf, 19, 0, DMA_Q(1, 3); DMA_Q(2, 10));
    WAIT_BAR(6);
    STEP(wbf, 1, true, wa, 1, 8, DMA_Q(3, 11); DMA_Q(4, 4));
    WAIT_BAR(6);
    STEP(wa, 2, true, wbf, 9, 2, DMA_Q(5, 5); DMA_Q(6, 12); DMA_Q(7, 13));
    WAIT_BAR(6);
    STEP(wbf, 3, true, wa, 17, 3, );
    {
        u32x4 t, vst;
        FOLD(t, 1.0f);
        auto s0 = __builtin_amdgcn_permlane32_swap(t[0], t[2], false, false);
        auto s1 = __builtin_amdgcn_permlane32_swap(t[1], t[3], false, false);
        vst[0] = s0[0]; vst[1] = s1[0]; vst[2] = s0[1]; vst[3] = s1[1];
        ST_SC1(vst, recV + lane * 16);
        ST1_SC1(nonce - csum(vst, 0), recV + 1024 + lane * 4);
#pragma unroll
        for (int i = 0; i < 4; ++i) { auto t_ = __builtin_amdgcn_permlane16_swap(vst[i], ONES, false, false); va4[i] = t_[0]; vb4[i] = t_[1]; }
        PIN(va4); PIN(vb4);
    }
    ac0 = f32x4{}; ac1 = f32x4{};
    const unsigned oKa = i0 <= 0 ? recK - 8 * REC + lane * 16 : WS_OOB, oKb = i0 <= 1 ? recK - 6 * REC + lane * 16 : WS_OOB;
    const unsigned oKc = i0 <= 2 ? recK - 4 * REC + lane * 16 : WS_OOB, oKd = i0 <= 3 ? recK - 2 * REC + lane * 16 : WS_OOB;
    const unsigned dC = 1024u - 12u * lane;
#define ALD4S(dst, off, so) asm volatile("buffer_load_dwordx4 %0, %1, %2, %3 offen sc1" : "=v"(dst) : "v"(off), "s"(rs), "s"(so) : "memory")
#define ALD1S(dst, off, so) asm volatile("buffer_load_dword %0, %1, %2, %3 offen sc1" : "=v"(dst) : "v"(off), "s"(rs), "s"(so) : "memory")
    unsigned cKa = oKa + dC, cKb = oKb + dC, cKc = oKc + dC, cKd = oKd + dC;
    PIN(cKa); PIN(cKb); PIN(cKc); PIN(cKd);
#define LOAD_K() do { ALD1(ck0, cKa, 0); ALD4(k0, oKa, 0); ALD1(ck1, cKb, 0); ALD4(k1, oKb, 0); \
                      ALD1(ck2, cKc, 0); ALD4(k2, oKc, 0); ALD1(ck3, cKd, 0); ALD4(k3, oKd, 0); } while (0)
#define LOAD_V() do { ALD1S(cv0, cKa, WS_V - WS_K); ALD4S(va0, oKa, WS_V - WS_K); ALD1S(cv1, cKb, WS_V - WS_K); ALD4S(va1, oKb, WS_V - WS_K); \
                      ALD1S(cv2, cKc, WS_V - WS_K); ALD4S(va2, oKc, WS_V - WS_K); ALD1S(cv3, cKd, WS_V - WS_K); ALD4S(va3, oKd, WS_V - WS_K); } while (0)
#define WAIT_K(N) asm volatile("s_waitcnt vmcnt(" #N ")" : "+v"(k0), "+v"(k1), "+v"(k2), "+v"(k3), "+v"(ck0), "+v"(ck1), "+v"(ck2), "+v"(ck3), "+v"(fw) :: "memory")
#define WAIT_V(N) asm volatile("s_waitcnt vmcnt(" #N ")" : "+v"(va0), "+v"(va1), "+v"(va2), "+v"(va3), "+v"(cv0), "+v"(cv1), "+v"(cv2), "+v"(cv3) :: "memory")
    WAIT_BAR(6);
    STEP(wa, 0, false, wbf, 10, 11, );
    WAIT_BAR(4);
    STEP(wbf, 1, false, wa, 4, 5, );
    u64 fw;
    WAIT_BAR(2);
    STEP(wa, 2, false, wbf, 12, 13, );
    asm volatile("global_load_dwordx2 %0, %1, off sc1" : "=v"(fw) : "v"(pfs) : "memory");
    LOAD_K();
    SB();
#pragma unroll
    for (int u = 0; u < 4; ++u) M1(wbf, 3, u, false);
    SB();
    bf16x8 qfo, qfx;
    {
        u32x4 t;
        FOLD(t, QSCALE);
        qfo = asbf(t);
        *(LAS u32x4*)(lds + L_R + w * 1024 + lane * 16) = t;
    }
    const int cb = g & 3, rot = idx >> 2;
    const unsigned voffO0 = (unsigned)(lane >> 4) * 1024u + (unsigned)((lane & 15) ^ ((lane >> 4) << 1)) * 16u;
    const unsigned voffO1 = (unsigned)(lane >> 4) * 1024u + (unsigned)((lane & 15) ^ ((4 + (lane >> 4)) << 1)) * 16u + 4096u;
    const char* ob = wo + (size_t)(8 * w) * 1024 + cb * 256;
#define DMA_O(c) do { const char* s_ = ob + (size_t)(((c) + rot) & 7) * 65536; \
        glds16(voffO0, s_, lds0 + (c) * 16384 + (unsigned)w * 2048); \
        glds16(voffO1, s_, lds0 + (c) * 16384 + (unsigned)w * 2048 + 1024); } while (0)
    BAR();
    qfx = *(LAS bf16x8*)(lds + L_R + (w ^ 1) * 1024 + lane * 16);
    const f32x16 zero = {};
    const float NINF = -__builtin_inff();
    f32x16 sc = MFMA(asbf(k4), qfo, zero);
    PIN(sc); SB();
    LOAD_V();
    DMA_O(0);
    WAIT_K(10);
    {
        const bool need = lane < 4 ? (b - 1 - lane >= 0) : (lane < 12);
        const unsigned want = (unsigned)(hsh(gself, lane) >> 32);
        if (__builtin_expect(__any(need && (unsigned)(fw >> 32) != want), 0)) {
#pragma unroll 1
            for (int spins = 0; spins < MAXSPIN; ++spins) {
                __builtin_amdgcn_s_sleep(1);
                asm volatile("global_load_dwordx2 %0, %1, off sc1\n\ts_waitcnt vmcnt(0)" : "=v"(fw) : "v"(pfs) : "memory");
                if (!__any(need && (unsigned)(fw >> 32) != want)) break;
            }
        }
    }
    const unsigned n0 = i0 <= 0 ? __builtin_amdgcn_readlane((unsigned)fw, 3) : 0u, n1 = i0 <= 1 ? __builtin_amdgcn_readlane((unsigned)fw, 2) : 0u;
    const unsigned n2 = i0 <= 2 ? __builtin_amdgcn_readlane((unsigned)fw, 1) : 0u, n3 = i0 <= 3 ? __builtin_amdgcn_readlane((unsigned)fw, 0) : 0u;
    const unsigned nO = __builtin_amdgcn_readlane((unsigned)fw, 4 + w);
#define K_BAD() __any((csum(k0, ck0) != n0) | (csum(k1, ck1) != n1) | (csum(k2, ck2) != n2) | (csum(k3, ck3) != n3))
    if (__builtin_expect(K_BAD(), 0)) {
#pragma unroll 1
        for (int spins = 0; spins < MAXSPIN; ++spins) {
            __builtin_amdgcn_s_sleep(1);
            LOAD_K(); WAIT_K(0);
            if (!K_BAD()) break;
        }
    }
    u32x4 kx;
#pragma unroll
    for (int i = 0; i < 4; ++i) kx[i] = mt ? k0[i] : k4[i];
    float mo = NINF, mx = NINF;
    if (i0 == 0) {
        f32x16 sa = MFMA(asbf(k0), qfo, zero);
        f32x16 sb = MFMA(asbf(k1), qfo, zero);
        f32x16 sd = MFMA(asbf(k1), qfx, zero);
        PIN(sa); PIN(sb); PIN(sd); SB();
#pragma unroll
        for (int i = 0; i < 16; ++i) sc[i] = __builtin_amdgcn_inverse_ballot_w64(diag_mask(i)) ? sc[i] : sa[i];
        PIN(sc); SB();
        sa = MFMA(asbf(kx), qfx, zero); PIN(sa);
        mo = max16(sc, mo); mo = max16(sb, mo); PIN(mo); SB();
        sb = MFMA(asbf(k2), qfo, zero); PIN(sb);
        mx = max16(sd, mx); PIN(mx); SB();
        sd = MFMA(asbf(k2), qfx, zero); PIN(sd);
        mx = max16(sa, mx); PIN(mx); SB();
        sa = MFMA(asbf(k3), qfo, zero); PIN(sa);
        mo = max16(sb, mo); PIN(mo); SB();
        sb = MFMA(asbf(k3), qfx, zero); PIN(sb);
        mx = max16(sd, mx); PIN(mx); SB();
        mo = max16(sa, mo);
        mx = max16(sb, mx);
    } else {
#pragma unroll
        for (int i = 0; i < 16; ++i) sc[i] = __builtin_amdgcn_inverse_ballot_w64(diag_mask(i)) ? sc[i] : NINF;
        mo = max16(sc, mo);
        if (mt == 0) { const f32x16 s = MFMA(asbf(kx), qfx, zero); mx = max16(s, mx); }
        if (i0 <= 1) { const f32x16 s1 = MFMA(asbf(k1), qfo, zero), s2 = MFMA(asbf(k1), qfx, zero); mo = max16(s1, mo); mx = max16(s2, mx); }
        if (i0 <= 2) { const f32x16 s1 = MFMA(asbf(k2), qfo, zero), s2 = MFMA(asbf(k2), qfx, zero); mo = max16(s1, mo); mx = max16(s2, mx); }
        if (i0 <= 3) { const f32x16 s1 = MFMA(asbf(k3), qfo, zero), s2 = MFMA(asbf(k3), qfx, zero); mo = max16(s1, mo); mx = max16(s2, mx); }
    }
    mo = hmerge(mo); mx = hmerge(mx);
    *(LAS f32x2*)(lds + L_E + w * 512 + lane * 8) = f32x2{mo, mx};
    WAIT_V(2);
#define V_BAD() __any((csum(va0, cv0) != n0) | (csum(va1, cv1) != n1) | (csum(va2, cv2) != n2) | (csum(va3, cv3) != n3))
    if (__builtin_expect(V_BAD(), 0)) {
#pragma unroll 1
        for (int spins = 0; spins < MAXSPIN; ++spins) {
            __builtin_amdgcn_s_sleep(1);
            LOAD_V(); WAIT_V(0);
            if (!V_BAD()) break;
        }
    }
#pragma unroll
    for (int i = 0; i < 4; ++i) {
        auto t0 = __builtin_amdgcn_permlane16_swap(va0[i], ONES, false, false); va0[i] = t0[0]; vb0[i] = t0[1];
        auto t1 = __builtin_amdgcn_permlane16_swap(va1[i], ONES, false, false); va1[i] = t1[0]; vb1[i] = t1[1];
        auto t2 = __builtin_amdgcn_permlane16_swap(va2[i], ONES, false, false); va2[i] = t2[0]; vb2[i] = t2[1];
        auto t3 = __builtin_amdgcn_permlane16_swap(va3[i], ONES, false, false); va3[i] = t3[0]; vb3[i] = t3[1];
    }
    u32x4 vxa, vxb;
#pragma unroll
    for (int i = 0; i < 4; ++i) { vxa[i] = mt ? va0[i] : va4[i]; vxb[i] = mt ? vb0[i] : vb4[i]; }
    PIN(vxa); PIN(vxb);
    BAR();
    {
        const f32x2 pm = *(LAS f32x2*)(lds + L_E + (w ^ 1) * 512 + lane * 8);
        mo = __builtin_fmaxf(mo, pm[1]); mx = __builtin_fmaxf(mx, pm[0]);
    }
    f32x16 nmx, nmo;
#pragma unroll
    for (int i = 0; i < 16; ++i) { nmx[i] = -mx; nmo[i] = -mo; }
    f32x16 ox = {}, o = {};
    u32x4 p0, p1, d0, d1;
#define COMBINED() do { _Pragma("unroll") for (int i = 0; i < 4; ++i) { \
            p0[i] = pk(__builtin_amdgcn_exp2f(sc[2 * i] - mo), __builtin_amdgcn_exp2f(sc[2 * i + 1] - mo)); \
            p1[i] = pk(__builtin_amdgcn_exp2f(sc[8 + 2 * i] - mo), __builtin_amdgcn_exp2f(sc[8 + 2 * i + 1] - mo)); \
            d0[i] = p0[i] & dm0[i]; d1[i] = p1[i] & dm1[i]; p0[i] ^= d0[i]; p1[i] ^= d1[i]; } } while (0)
    const lptr exw = lds + L_E + 4096 + w * 3072;
    if (i0 == 0) {
        u32x4 a0, a1, b0, b1;
        f32x16 sa = MFMA(asbf(kx), qfx, nmx); PIN(sa);
        COMBINED(); PIN(p0); PIN(p1); PIN(d0); PIN(d1); SB();
        f32x16 sb = MFMA(asbf(k1), qfx, nmx); PIN(sb);
        PV(o, va4, vb4, d0, d1); PV(o, va0, vb0, p0, p1); PIN(o); DMA_O(1);
        exp_tile(sa, a0, a1); PIN(a0); PIN(a1); SB();
        sa = MFMA(asbf(k2), qfx, nmx); PIN(sa);
        PV(ox, vxa, vxb, a0, a1); PIN(ox); DMA_O(2);
        exp_tile(sb, b0, b1); PIN(b0); PIN(b1); SB();
        sb = MFMA(asbf(k3), qfx, nmx); PIN(sb);
        PV(ox, va1, vb1, b0, b1); PIN(ox); DMA_O(3);
        exp_tile(sa, a0, a1); PIN(a0); PIN(a1); SB();
        sa = MFMA(asbf(k1), qfo, nmo); PIN(sa);
        PV(ox, va2, vb2, a0, a1); PIN(ox); DMA_O(4);
        exp_tile(sb, b0, b1); PIN(b0); PIN(b1); SB();
        sb = MFMA(asbf(k2), qfo, nmo); PIN(sb);
        PV(ox, va3, vb3, b0, b1); PIN(ox); DMA_O(5);
        exp_tile(sa, a0, a1); PIN(a0); PIN(a1); SB();
        sa = MFMA(asbf(k3), qfo, nmo); PIN(sa);
        PV(o, va1, vb1, a0, a1); PIN(o); DMA_O(6);
        *(LAS f32x4*)(exw + lane * 16) = f32x4{ox[0], ox[1], ox[2], ox[3]};
        *(LAS f32x4*)(exw + lane * 16 + 1024) = f32x4{ox[4], ox[5], ox[6], ox[7]};
        *(LAS float*)(exw + 2048 + lane * 4) = ox[8];
        exp_tile(sb, b0, b1); PIN(b0); PIN(b1); SB();
        PV(o, va2, vb2, b0, b1); PIN(o); DMA_O(7);
        exp_tile(sa, a0, a1);
        PV(o, va3, vb3, a0, a1);
    } else {
        DMA_O(1); DMA_O(2); DMA_O(3); DMA_O(4); DMA_O(5); DMA_O(6); DMA_O(7);
        COMBINED();
        PV(o, va4, vb4, d0, d1);
        u32x4 a0, a1;
        if (mt == 0) { const f32x16 s = MFMA(asbf(kx), qfx, nmx); exp_tile(s, a0, a1); PV(ox, va4, vb4, a0, a1); }
        if (i0 <= 1) { const f32x16 s = MFMA(asbf(k1), qfx, nmx), t = MFMA(asbf(k1), qfo, nmo); exp_tile(s, a0, a1); PV(ox, va1, vb1, a0, a1); exp_tile(t, a0, a1); PV(o, va1, vb1, a0, a1); }
        if (i0 <= 2) { const f32x16 s = MFMA(asbf(k2), qfx, nmx), t = MFMA(asbf(k2), qfo, nmo); exp_tile(s, a0, a1); PV(ox, va2, vb2, a0, a1); exp_tile(t, a0, a1); PV(o, va2, vb2, a0, a1); }
        if (i0 <= 3) { const f32x16 s = MFMA(asbf(k3), qfx, nmx), t = MFMA(asbf(k3), qfo, nmo); exp_tile(s, a0, a1); PV(ox, va3, vb3, a0, a1); exp_tile(t, a0, a1); PV(o, va3, vb3, a0, a1); }
        *(LAS f32x4*)(exw + lane * 16) = f32x4{ox[0], ox[1], ox[2], ox[3]};
        *(LAS f32x4*)(exw + lane * 16 + 1024) = f32x4{ox[4], ox[5], ox[6], ox[7]};
        *(LAS float*)(exw + 2048 + lane * 4) = ox[8];
    }
    BAR();
    {
        const lptr s = lds + L_E + 4096 + (w ^ 1) * 3072;
        const f32x4 a = *(LAS f32x4*)(s + lane * 16), c = *(LAS f32x4*)(s + lane * 16 + 1024);
        const float lp = *(LAS float*)(s + 2048 + lane * 4);
        const float inv = __builtin_amdgcn_rcpf(o[8] + lp);
        u32x4 t;
        t[0] = pk((o[0] + a[0]) * inv, (o[1] + a[1]) * inv); t[1] = pk((o[2] + a[2]) * inv, (o[3] + a[3]) * inv);
        t[2] = pk((o[4] + c[0]) * inv, (o[5] + c[1]) * inv); t[3] = pk((o[6] + c[2]) * inv, (o[7] + c[3]) * inv);
        const unsigned recO = WS_O + (unsigned)(qo * 32 + head) * REC;
        ST_SC1(t, recO + lane * 16);
        ST1_SC1(nonce - csum(t, 0), recO + 1024 + lane * 4);
    }
    WAIT_BAR(2);
    const int qb3 = 2 * b + (g >> 2);
    bf16x8 wf[16];
    {
        const int rl = 16 * (g4 >> 1) + 4 * (g4 & 1) + q4;
        lptr base = lds + rl * 256 + 32 * (w ^ (rl & 7)) + 8 * p4, base1 = base + 65536;
        PIN(base1);
#pragma unroll
        for (int t = 0; t < 16; ++t) wf[t] = trd2((t < 8 ? base : base1) + (t & 7) * 8192, 2048);
    }
    {
        const unsigned rO = WS_O + (unsigned)(qb3 * 32 + 4 * w) * REC + lane * 16;
        const unsigned i_0 = cb & 3, i_1 = (cb + 1) & 3, i_2 = (cb + 2) & 3, i_3 = (cb + 3) & 3;
        u32x4 f0, f1, f2, f3; unsigned c0, c1, c2, c3;
#pragma unroll 1
        for (int spins = 0; spins < MAXSPIN; ++spins) {
            ALD1(c0, rO + i_0 * REC + dC, 0); ALD4(f0, rO + i_0 * REC, 0);
            asm volatile("s_waitcnt vmcnt(0)" : "+v"(f0), "+v"(c0) :: "memory");
            if (!__any(csum(f0, c0) != nO)) break;
            __builtin_amdgcn_s_sleep(1);
        }
#pragma unroll 1
        for (int spins = 0; spins < MAXSPIN; ++spins) {
            ALD1(c1, rO + i_1 * REC + dC, 0); ALD4(f1, rO + i_1 * REC, 0);
            ALD1(c2, rO + i_2 * REC + dC, 0); ALD4(f2, rO + i_2 * REC, 0);
            ALD1(c3, rO + i_3 * REC + dC, 0); ALD4(f3, rO + i_3 * REC, 0);
            asm volatile("s_waitcnt vmcnt(0)" : "+v"(f1), "+v"(c1), "+v"(f2), "+v"(c2), "+v"(f3), "+v"(c3) :: "memory");
            if (!__any((csum(f1, c1) != nO) | (csum(f2, c2) != nO) | (csum(f3, c3) != nO))) break;
            __builtin_amdgcn_s_sleep(1);
        }
        const lptr d = lds + L_E + (4 * ((w - rot) & 7)) * 1024 + lane * 16;
        *(LAS u32x4*)(d + i_0 * 1024) = f0; *(LAS u32x4*)(d + i_1 * 1024) = f1; *(LAS u32x4*)(d + i_2 * 1024) = f2; *(LAS u32x4*)(d + i_3 * 1024) = f3;
    }
    BAR();
    ac0 = f32x4{}; ac1 = f32x4{};
    {
        const lptr ob2 = lds + L_E + (g4 >> 1) * 1024 + (c16 + 32 * (g4 & 1)) * 16;
#pragma unroll
        for (int t = 0; t < 16; ++t) {
            const bf16x8 a = *(LAS bf16x8*)(ob2 + t * 2048), c = *(LAS bf16x8*)(ob2 + t * 2048 + 256);
            ac0 = MFMA16(wf[t], a, ac0); ac1 = MFMA16(wf[t], c, ac1);
        }
        u32x4 t;
        FOLD(t, 1.0f);
        *(u32x4*)(out + (size_t)(32 * qb3 + r) * 1024 + (128 * cb + 16 * w + 8 * hh) * 2) = t;
    }
    if (tid == 0) {
        u64* pg = (u64*)(ws + WS_G + self * 128);
        const u64 val = hsh(gself, 1000);
        asm volatile("global_store_dwordx2 %0, %1, off sc1" :: "v"(pg), "v"(val) : "memory");
    }
}

extern "C" void kernel_launch(void* const* d_in, const int* in_sizes, int n_in,
                              void* d_out, int out_size, void* d_ws, size_t ws_size,
                              hipStream_t stream) {
    (void)hipFuncSetAttribute((const void*)fused_kernel, hipFuncAttributeMaxDynamicSharedMemorySize, LDS_BYTES);
    fused_kernel<<<256, 512, LDS_BYTES, stream>>>((const char*)d_in[0], (const char*)d_in[1], (const char*)d_in[2], (char*)d_out, (char*)d_ws);
}
```

```cpp
#define CODEPF
#include <hip/hip_runtime.h>
#include <hip/hip_bf16.h>
#include <stdint.h>

typedef __attribute__((ext_vector_type(8))) short bf16x8;
typedef __attribute__((ext_vector_type(4))) short s16x4;
typedef __attribute__((ext_vector_type(16))) float f32x16;
typedef __attribute__((ext_vector_type(4))) float f32x4;
typedef __attribute__((ext_vector_type(2))) float f32x2;
typedef __attribute__((ext_vector_type(4))) unsigned u32x4;
typedef __attribute__((ext_vector_type(2))) unsigned u32x2;
typedef __attribute__((ext_vector_type(2))) __bf16 bf16x2;
typedef __attribute__((ext_vector_type(2))) unsigned short u16x2;
typedef unsigned long long u64;
#define LAS __attribute__((address_space(3)))
#define DI __device__ __forceinline__
typedef LAS char* lptr;

constexpr unsigned REC2 = 2560, REC = 1280;
constexpr unsigned WS_K = 0, WS_V = 1024 * REC2, WS_O = 2048 * REC2, WS_G = WS_O + 2048 * REC, WS_F = WS_G + 256 * 128, WS_END = WS_F + 256 * 128;
constexpr unsigned WS_OOB = 0x7FFFF000u;
constexpr unsigned HS = 8192;
constexpr unsigned L_Q = 0 * HS, L_H = 1 * HS, L_K = 2 * HS;
constexpr unsigned L_N = 4 * HS, L_Y = 4 * HS + 4096;
constexpr unsigned L_E = 0 * HS;
constexpr unsigned LDS_BYTES = 20 * HS;
#define KSLEEP 4
#define OSLEEP 5
#define CPF 4
constexpr unsigned CODE_BYTES = 21 * 1024;
constexpr int MAXSPIN = 1 << 15;
constexpr float QSCALE = 0.25f * 1.4426950408889634f;
constexpr unsigned ONES = 0x3F803F80u;
constexpr int WOS[16] = {12, 13, 14, 15, 16, 18, 19, 17, 5, 6, 7, 8, 9, 10, 11, 4};

#define MFMA(a, b, c) __builtin_amdgcn_mfma_f32_32x32x16_bf16((a), (b), (c), 0, 0, 0)
#define MFMA16(a, b, c) __builtin_amdgcn_mfma_f32_16x16x32_bf16((a), (b), (c), 0, 0, 0)
#define SB() __builtin_amdgcn_sched_barrier(0)
#define PIN(x) asm volatile("" : "+v"(x))
#define PINS(x) asm volatile("" : "+s"(x))
#define FENCE() asm volatile("" ::: "memory")
constexpr int wcnt(int vm, int lgkm) { return (vm & 15) | (7 << 4) | (lgkm << 8) | ((vm >> 4) << 14); }
#define WAIT_BAR(N) do { FENCE(); __builtin_amdgcn_s_waitcnt(wcnt(N, 0)); __builtin_amdgcn_s_barrier(); FENCE(); SB(); } while (0)
#define WAIT_BARV(N) do { FENCE(); __builtin_amdgcn_s_waitcnt(wcnt(N, 15)); __builtin_amdgcn_s_barrier(); FENCE(); SB(); } while (0)
#define WAIT_BARL(L) do { FENCE(); __builtin_amdgcn_s_waitcnt(wcnt(63, L)); __builtin_amdgcn_s_barrier(); FENCE(); SB(); } while (0)
#define BARX() do { FENCE(); __builtin_amdgcn_s_barrier(); FENCE(); SB(); } while (0)
#define BAR() WAIT_BAR(63)
#define MX3(a, b, c) __builtin_fmaxf(__builtin_fmaxf((a), (b)), (c))
#define ST_SC1(v, off) __builtin_amdgcn_raw_buffer_store_b128((v), rs, (off), 0, 16)
#define ST1_SC1(v, off) __builtin_amdgcn_raw_buffer_store_b32((v), rs, (off), 0, 16)
#define ALD4(dst, off) asm volatile("buffer_load_dwordx4 %0, %1, %2, 0 offen sc1" : "=v"(dst) : "v"(off), "s"(rs) : "memory")
#define ALD1(dst, off) asm volatile("buffer_load_dword %0, %1, %2, 0 offen sc1" : "=v"(dst) : "v"(off), "s"(rs) : "memory")
#define ALD4S(dst, off, so) asm volatile("buffer_load_dwordx4 %0, %1, %2, %3 offen sc1" : "=v"(dst) : "v"(off), "s"(rs), "s"(so) : "memory")
#define ALD1S(dst, off, so) asm volatile("buffer_load_dword %0, %1, %2, %3 offen sc1" : "=v"(dst) : "v"(off), "s"(rs), "s"(so) : "memory")

#define KATTR

DI unsigned pk(float a, float b) { f32x2 v = {a, b}; bf16x2 r = __builtin_convertvector(v, bf16x2); return __builtin_bit_cast(unsigned, r); }
DI void glds16(unsigned voff, const char* sbase, unsigned lds_dst) {
    asm volatile("s_mov_b32 m0, %2\n\ts_nop 0\n\tglobal_load_lds_dwordx4 %0, %1" :: "v"(voff), "s"(sbase), "s"(lds_dst) : "memory");
}
DI s16x4 trd(lptr p) { return __builtin_amdgcn_ds_read_tr16_b64_v4i16((LAS s16x4*)p); }
DI bf16x8 trd2(lptr p, int off2) { s16x4 a = trd(p), b = trd(p + off2); return __builtin_shufflevector(a, b, 0, 1, 2, 3, 4, 5, 6, 7); }
DI u64 hsh(u64 g, unsigned slot) { return g * 0x9E3779B97F4A7C15ull + (u64)(2u * slot + 1u) * 0xD1B54A32D192ED03ull; }
DI bf16x8 asbf(u32x4 v) { return __builtin_bit_cast(bf16x8, v); }
DI unsigned d2(unsigned a, unsigned k, unsigned c) { return __builtin_amdgcn_udot2(__builtin_bit_cast(u16x2, a), __builtin_bit_cast(u16x2, k), c, false); }
DI unsigned csum(u32x4 v, unsigned c) { return d2(v.x, 0x682f978bu, d2(v.y, 0x92e1a2d1u, d2(v.z, 0x4ca14349u, d2(v.w, 0xf6314f97u, c)))); }
DI float max16(const f32x16& s, float m) {
#pragma unroll
    for (int i = 0; i < 16; i += 2) m = MX3(m, s[i], s[i + 1]);
    return m;
}
DI float hmerge(float m) { auto rr = __builtin_amdgcn_permlane32_swap(__float_as_uint(m), __float_as_uint(m), false, false);
    return __builtin_fmaxf(__uint_as_float(rr[0]), __uint_as_float(rr[1])); }
constexpr int crow(int r, int hi) { return (r & 3) + 8 * (r >> 2) + 4 * hi; }
constexpr u64 diag_mask(int reg) { u64 m = 0; for (int l = 0; l < 64; ++l) if (crow(reg, l >> 5) <= (l & 31)) m |= 1ull << l; return m; }
DI f32x16 bcast(float v) { f32x16 r;
#pragma unroll
    for (int i = 0; i < 16; ++i) r[i] = v;
    return r; }
DI f32x16 bcast2(float v) {
    const f32x2 p = {v, v}; f32x2 q[8]; q[0] = p;
#pragma unroll
    for (int i = 1; i < 8; ++i) asm volatile("v_mov_b64 %0, %1" : "=v"(q[i]) : "v"(p));
    f32x16 r;
#pragma unroll
    for (int i = 0; i < 16; ++i) r[i] = q[i >> 1][i & 1];
    return r; }
DI unsigned dppx1(unsigned v) { return __builtin_amdgcn_update_dpp(0, v, 0xB1, 0xF, 0xF, true); }
DI u32x4 fill4(unsigned v) {
    const u64 p = ((u64)v << 32) | v; u32x2 a, b;
    asm volatile("v_mov_b64 %0, %1" : "=v"(a) : "s"(p)); asm volatile("v_mov_b64 %0, %1" : "=v"(b) : "s"(p));
    return u32x4{a[0], a[1], b[0], b[1]}; }
#define CS5(c, K) do { e0 = d2(v0.c, K, e0); e1 = d2(v1.c, K, e1); e2 = d2(v2.c, K, e2); e3 = d2(v3.c, K, e3); e4 = d2(v4.c, K, e4); asm volatile("" : "+v"(e0), "+v"(e1), "+v"(e2), "+v"(e3), "+v"(e4)); } while (0)
DI void csum5(unsigned& e0, unsigned& e1, unsigned& e2, unsigned& e3, unsigned& e4, const u32x4& v0, const u32x4& v1, const u32x4& v2, const u32x4& v3, const u32x4& v4) {
    CS5(w, 0xf6314f97u); CS5(z, 0x4ca14349u); CS5(y, 0x92e1a2d1u); CS5(x, 0x682f978bu); }
#define CSUM4(e0, e1, e2, e3, v0, v1, v2, v3) do { \
    e0 = d2(v0.w, 0xf6314f97u, e0); e1 = d2(v1.w, 0xf6314f97u, e1); e2 = d2(v2.w, 0xf6314f97u, e2); e3 = d2(v3.w, 0xf6314f97u, e3); asm volatile("" : "+v"(e0), "+v"(e1), "+v"(e2), "+v"(e3)); \
    e0 = d2(v0.z, 0x4ca14349u, e0); e1 = d2(v1.z, 0x4ca14349u, e1); e2 = d2(v2.z, 0x4ca14349u, e2); e3 = d2(v3.z, 0x4ca14349u, e3); asm volatile("" : "+v"(e0), "+v"(e1), "+v"(e2), "+v"(e3)); \
    e0 = d2(v0.y, 0x92e1a2d1u, e0); e1 = d2(v1.y, 0x92e1a2d1u, e1); e2 = d2(v2.y, 0x92e1a2d1u, e2); e3 = d2(v3.y, 0x92e1a2d1u, e3); asm volatile("" : "+v"(e0), "+v"(e1), "+v"(e2), "+v"(e3)); \
    e0 = d2(v0.x, 0x682f978bu, e0); e1 = d2(v1.x, 0x682f978bu, e1); e2 = d2(v2.x, 0x682f978bu, e2); e3 = d2(v3.x, 0x682f978bu, e3); asm volatile("" : "+v"(e0), "+v"(e1), "+v"(e2), "+v"(e3)); } while (0)

DI void exp_tile(const f32x16& s, u32x4& p0, u32x4& p1) {
#pragma unroll
    for (int i = 0; i < 4; ++i) p0[i] = pk(__builtin_amdgcn_exp2f(s[2 * i]), __builtin_amdgcn_exp2f(s[2 * i + 1]));
#pragma unroll
    for (int i = 0; i < 4; ++i) p1[i] = pk(__builtin_amdgcn_exp2f(s[8 + 2 * i]), __builtin_amdgcn_exp2f(s[8 + 2 * i + 1]));
}
#define PV(o, va, vb, p0, p1) do { o = MFMA(asbf(va), asbf(p0), o); o = MFMA(asbf(vb), asbf(p1), o); } while (0)
DI u64 part_bad(const f32x16& o) {
    f32x2 z = (f32x2{o[0], o[1]} + f32x2{o[2], o[3]}) + (f32x2{o[4], o[5]} + f32x2{o[6], o[7]});
    float t;
    asm("v_fma_f32 %0, %1, 0, %2" : "=v"(t) : "v"(z[0]), "v"(o[8]));
    asm("v_fma_f32 %0, %1, 0, %2" : "=v"(t) : "v"(z[1]), "v"(t));
    return __builtin_amdgcn_ballot_w64(!(t < 0x1p120f));
}

__global__ void __launch_bounds__(512, 2) KATTR
fused_kernel(const char* __restrict__ x, const char* __restrict__ wqkv, const char* __restrict__ wo, char* __restrict__ out, char* ws) {
    extern __shared__ __attribute__((aligned(16))) char smem[];
    const lptr lds = (lptr)smem;
    const unsigned lds0 = (unsigned)(uintptr_t)smem;
    const char* pc0;
    asm volatile("s_getpc_b64 %0" : "=s"(pc0));
    const int tid = threadIdx.x, lane = tid & 63;
    const int w = __builtin_amdgcn_readfirstlane(tid >> 6);
    const int id = blockIdx.x, xcd = id & 7, idx = id >> 3;
    const int b = 8 * (xcd >> 1) + (idx >> 2), g = 4 * (xcd & 1) + (idx & 3);
    const int rho0 = ((idx >> 2) + 2 * (idx & 3)) & 7;
    const int d = w & 3;
    const unsigned voffX = (unsigned)(lane >> 3) * 1024u + (unsigned)((lane & 7) ^ ((4 * d + (lane >> 4)) & 7)) * 16u;
    const unsigned voffW = (unsigned)(lane >> 3) * 3072u + (unsigned)((lane & 7) ^ (((lane >> 4) & 1) << 2)) * 16u;
    const unsigned ldd = lds0 + (unsigned)d * 1024;
    asm volatile("" :: "s"(x), "s"(wqkv), "s"(wo), "s"(out), "s"(ws));
    const char* xb = x + (size_t)(64 * b + 8 * d) * 1024;
    const char* wb = wqkv + (size_t)(8 * d) * 3072 + g * 128;
#define SX(p) (4 * ((p) >> 1) + ((p) & 1))
#define SK(p) (4 * ((p) >> 1) + 2 + ((p) & 1))
#define DMA_X(p) do { PINS(rho); const char* s_ = xb + (((p) + rho) & 7) * 128; glds16(voffX, s_, ldd + SX(p) * HS); glds16(voffX, s_ + 32768, ldd + SX(p) * HS + 4096); } while (0)
#define DMA_W(matoff, p, hs) do { PINS(rho); const char* s_ = wb + (size_t)(((p) + rho) & 7) * (64 * 3072) + (matoff); glds16(voffW, s_, ldd + (hs) * HS); glds16(voffW, s_ + 32 * 3072, ldd + (hs) * HS + 4096); } while (0)
#define DMA_K(p) DMA_W(1024, p, SK(p))
#define DMA_V(p, hs) DMA_W(2048, p, hs)
#define DMA_Q(p, hs) DMA_W(0, p, hs)
    if (w < 4) {
        int rho = rho0;
        DMA_X(0); DMA_K(0); DMA_X(1); DMA_K(1); DMA_V(0, 16); DMA_Q(0, 17);
    }
    SB();
    const int r = lane & 31, hh = lane >> 5;
    const int mt = w & 1, j = (w >> 1) & 1, kh = w >> 2;
    const int head = 4 * g + 2 * j + kh;
    const int qo = 2 * b + mt;
    const int self = b * 8 + g;
    const int i0 = b < 4 ? 4 - b : 0;
    const __amdgpu_buffer_rsrc_t rs = __builtin_amdgcn_make_buffer_rsrc(ws, 0, WS_END, 0x00020000);
    const unsigned recK = WS_K + (unsigned)((2 * g + j) * 64 + qo) * REC2;
    const unsigned recO = WS_O + (unsigned)(qo * 32 + head) * REC;
    const int cb = g & 3, qb3 = 2 * b + (g >> 2), rot = idx >> 2;
    const int cons = lane < 4 ? (b + 1 + lane) * 8 + g : b * 8 + ((lane - 4) & 7);
    const bool cons_ok = lane < 4 ? (b + 1 + lane < 32) : (lane < 12);
    const unsigned cslot = lane < 4 ? lane : 4 + g;
    u64 gcons;
    {
        const char* pgc = ws + WS_G + (cons_ok ? cons : self) * 128;
        asm volatile("global_load_dwordx2 %0, %1, off sc1" : "=v"(gcons) : "v"(pgc) : "memory");
    }
    unsigned dummy = 0;
    if (kh) {
        const char* pp = wo + (size_t)(64 * rot + 16 * d + ((lane >> 1) & 15)) * 1024 + cb * 256 + (lane & 1) * 128;
        const unsigned ln = (unsigned)(idx * 8 + d * CPF + (lane & (CPF - 1))) % (CODE_BYTES / 128);
        if (lane >= 32) pp = pc0 + ln * 128;
        if (lane >= 32 && lane < 32 + CPF)
        asm volatile("global_load_dword %0, %1, off" : "=v"(dummy) : "v"(pp) : "memory");
    }
#define CW0 1
    const int c16 = lane & 15, g4 = lane >> 4, q4 = c16 >> 2, p4 = lane & 3;
    u32x4 k0, k1, k2, k3, k4, va0, va1, va2, va3, va4, vb0, vb1, vb2, vb3, vb4;
    unsigned ck0, ck1, ck2, ck3, cv0, cv1, cv2, cv3;
    u64 fw;
    bf16x8 qfo, qfx;
    const char* pfs = ws + WS_F + (unsigned)self * 128 + (lane < 12 ? lane : 0) * 8;
    const bool needF = lane < 4 ? (b - 1 - lane >= 0) : (lane < 12);
    const unsigned oP = recK + kh * 1024 + lane * 16, oC = recK + 2048 + lane * 8 + kh * 4;
    const unsigned oK0 = i0 <= 0 ? oP - 8 * REC2 : WS_OOB, oK1 = i0 <= 1 ? oP - 6 * REC2 : WS_OOB, oK2 = i0 <= 2 ? oP - 4 * REC2 : WS_OOB, oK3 = i0 <= 3 ? oP - 2 * REC2 : WS_OOB;
    const unsigned cK0 = i0 <= 0 ? oC - 8 * REC2 : WS_OOB, cK1 = i0 <= 1 ? oC - 6 * REC2 : WS_OOB, cK2 = i0 <= 2 ? oC - 4 * REC2 : WS_OOB, cK3 = i0 <= 3 ? oC - 2 * REC2 : WS_OOB;
#define LOAD_K() do { ALD1(ck3, cK3); ALD4(k3, oK3); ALD1(ck0, cK0); ALD4(k0, oK0); ALD1(ck1, cK1); ALD4(k1, oK1); ALD1(ck2, cK2); ALD4(k2, oK2); } while (0)
#define LOAD_V() do { ALD1S(cv3, cK3, WS_V - WS_K); ALD4S(va3, oK3, WS_V - WS_K); ALD1S(cv0, cK0, WS_V - WS_K); ALD4S(va0, oK0, WS_V - WS_K); \
                      ALD1S(cv1, cK1, WS_V - WS_K); ALD4S(va1, oK1, WS_V - WS_K); ALD1S(cv2, cK2, WS_V - WS_K); ALD4S(va2, oK2, WS_V - WS_K); } while (0)
#define WAIT_K(N) asm volatile("s_waitcnt vmcnt(" #N ")" : "+v"(k0), "+v"(k1), "+v"(k2), "+v"(k3), "+v"(ck0), "+v"(ck1), "+v"(ck2), "+v"(ck3), "+v"(fw) :: "memory")
#define WAIT_V(N) asm volatile("s_waitcnt vmcnt(" #N ")" : "+v"(va0), "+v"(va1), "+v"(va2), "+v"(va3), "+v"(cv0), "+v"(cv1), "+v"(cv2), "+v"(cv3) :: "memory")
#define LOAD_F() asm volatile("global_load_dwordx2 %0, %1, off sc1" : "=v"(fw) : "v"(pfs) : "memory")
#define UNF(va, vb) do { _Pragma("unroll") for (int i = 0; i < 4; ++i) { auto t_ = __builtin_amdgcn_permlane16_swap(va[i], vb[i], false, false); va[i] = t_[0]; vb[i] = t_[1]; } PIN(va); PIN(vb); } while (0)
    u32x4 dm0, dm1;
#pragma unroll
    for (int i = 0; i < 4; ++i) {
        dm0[i] = (crow(2 * i, 0) + 4 * hh <= r ? 0xFFFFu : 0u) | (crow(2 * i + 1, 0) + 4 * hh <= r ? 0xFFFF0000u : 0u);
        dm1[i] = (crow(8 + 2 * i, 0) + 4 * hh <= r ? 0xFFFFu : 0u) | (crow(8 + 2 * i + 1, 0) + 4 * hh <= r ? 0xFFFF0000u : 0u);
    }
    PIN(dm0); PIN(dm1);
    if (w < 4) {
        int rho = rho0;
        DMA_X(2); DMA_K(2); DMA_X(3); DMA_K(3);
    }
    const unsigned voffO = (unsigned)(lane >> 4) * 1024u + (unsigned)((lane & 15) ^ ((4 * (w & 1) + (lane >> 4)) << 1)) * 16u;
#define DMA_O2(h) do { PINS(rot_); const char* s_ = ob + (size_t)((((h) >> 1) + rot_) & 7) * 65536 + ((h) & 1) * 32768; \
        glds16(voffO, s_, ldd + WOS[h] * HS); glds16(voffO, s_ + 16384, ldd + WOS[h] * HS + 4096); } while (0)
    lptr wr0 = lds + (8 * hh + q4) * 128 + 64 * (j ^ (q4 >> 1)) + 32 * ((lane >> 4) & 1) + 8 * p4;
    lptr wr1 = wr0 + 8 * HS, wr2 = wr0 + 16 * HS;
    PIN(wr0); PIN(wr1); PIN(wr2);
    lptr xr[4], xs[4];
#pragma unroll
    for (int i = 0; i < 4; ++i) { xr[i] = lds + (32 * mt + r) * 128 + (((2 * i + hh) ^ ((r >> 1) & 7)) * 16); xs[i] = xr[i] + 8 * HS; PIN(xr[i]); PIN(xs[i]); }
    unsigned foff = (w == 4 && cons_ok) ? WS_F + cons * 128 + cslot * 8 : WS_OOB; PIN(foff);
    unsigned sC = (lane & 1) ? WS_OOB : recK + 2048 + lane * 8; PIN(sC);
    if (w < 4) __builtin_amdgcn_s_waitcnt(wcnt(8, 15)); else __builtin_amdgcn_s_waitcnt(wcnt(CW0, 15));
    FENCE();
    PIN(gcons);
    const u64 gself = ((u64)(unsigned)__builtin_amdgcn_readlane((unsigned)(gcons >> 32), 12) << 32) | (unsigned)__builtin_amdgcn_readlane((unsigned)gcons, 12);
    const unsigned nonce = (unsigned)(hsh(gself, 77) >> 32);
    unsigned fn;
    if (w < 4) {
        int rho = rho0;
        BARX();
        DMA_X(4); DMA_K(4); DMA_X(5); DMA_K(5);
        WAIT_BARV(8);
        DMA_X(6); DMA_K(6); DMA_X(7); DMA_K(7);
        WAIT_BARV(8);
        DMA_V(1, 18); DMA_V(2, 19); DMA_V(3, 0); DMA_V(4, 1);
        WAIT_BARV(8);
        DMA_V(5, 2); DMA_V(6, 3); DMA_V(7, 4); DMA_Q(1, 5);
        WAIT_BARV(14);
        DMA_Q(2, 6); DMA_Q(3, 7); DMA_Q(4, 8); DMA_Q(5, 9);
        WAIT_BARV(18);
        DMA_Q(6, 10); DMA_Q(7, 11);
        int rot_ = rot;
        const char* ob = wo; PINS(ob); ob += (size_t)(4 * d) * 1024 + cb * 256;
        const unsigned wantF = (unsigned)(hsh(gself, lane) >> 32);
        vb0 = fill4(i0 <= 0 ? ONES : 0u); vb1 = fill4(i0 <= 1 ? ONES : 0u); vb2 = fill4(i0 <= 2 ? ONES : 0u); vb3 = fill4(i0 <= 3 ? ONES : 0u); vb4 = fill4(ONES);
        WAIT_BARV(18);
        DMA_O2(0); DMA_O2(1);
        WAIT_BARV(18);
        DMA_O2(2); DMA_O2(3);
        WAIT_BARV(20);
        DMA_O2(4); DMA_O2(5); DMA_O2(6);
        WAIT_BARV(14);
        u32x4 m0, m1, m2, m3; u32x2 c0, c1, c2, c3;
#define ALD2(dst, off) asm volatile("buffer_load_dwordx2 %0, %1, %2, 0 offen sc1" : "=v"(dst) : "v"(off), "s"(rs) : "memory")
#define ALD4I(dst, off) asm volatile("buffer_load_dwordx4 %0, %1, %2, 0 offen offset:1024 sc1" : "=v"(dst) : "v"(off), "s"(rs) : "memory")
        __builtin_amdgcn_s_sleep(KSLEEP);
#define CSUM2(e0, e1, v0, v1) do { e0 = d2(v0.w, 0xf6314f97u, e0); e1 = d2(v1.w, 0xf6314f97u, e1); e0 = d2(v0.z, 0x4ca14349u, e0); e1 = d2(v1.z, 0x4ca14349u, e1); \
            e0 = d2(v0.y, 0x92e1a2d1u, e0); e1 = d2(v1.y, 0x92e1a2d1u, e1); e0 = d2(v0.x, 0x682f978bu, e0); e1 = d2(v1.x, 0x682f978bu, e1); } while (0)
#define KTILE(N, c, m, k, pos, ln) do { asm volatile("s_waitcnt vmcnt(" #N ")" : "+v"(k), "+v"(m), "+v"(c) :: "memory"); \
            *(LAS u32x4*)(lds + L_K + d * 4096 + (pos) * 1024 + lane * 16) = m; CSUM2(c[0], c[1], k, m); \
            const unsigned n_ = __builtin_amdgcn_readlane(fn, ln); badk |= __builtin_amdgcn_ballot_w64((c[0] != n_) | (c[1] != n_)); SB(); } while (0)
#pragma unroll 1
        for (int spins = 0; spins < MAXSPIN; ++spins) {
            LOAD_F();
            ALD2(c3, cK3); ALD4I(m3, oK3); ALD4(k3, oK3); ALD2(c0, cK0); ALD4I(m0, oK0); ALD4(k0, oK0);
            ALD2(c1, cK1); ALD4I(m1, oK1); ALD4(k1, oK1); ALD2(c2, cK2); ALD4I(m2, oK2); ALD4(k2, oK2);
            asm volatile("s_waitcnt vmcnt(12)" : "+v"(fw) :: "memory");
            fn = needF ? (unsigned)fw : 0u;
            *(LAS unsigned*)(lds + L_N + d * 256 + lane * 4) = fn;
            u64 badk = __builtin_amdgcn_ballot_w64(needF && (unsigned)(fw >> 32) != wantF);
            SB();
            KTILE(9, c3, m3, k3, 3, 0); KTILE(6, c0, m0, k0, 0, 3); KTILE(3, c1, m1, k1, 1, 2); KTILE(0, c2, m2, k2, 2, 1);
            if (!badk) break;
            __builtin_amdgcn_s_sleep(2);
        }
        WAIT_BARL(0);
        qfo = *(LAS bf16x8*)(lds + L_Q + d * 2048 + lane * 16);
        k4 = *(LAS u32x4*)(lds + L_H + d * 2048 + lane * 16);
        va4 = *(LAS u32x4*)(lds + L_H + d * 2048 + 1024 + lane * 16);
    } else {
        __builtin_amdgcn_s_setprio(2);
        u64 fval = (hsh(gcons, cslot) & 0xFFFFFFFF00000000ull) | nonce; PIN(fval);
        bf16x8 xf[32], wa[8];
        f32x16 accA = {}, accB = {};
#define WRP(hs) (((hs) < 8 ? wr0 : ((hs) < 16 ? wr1 : wr2)) + ((hs) & 7) * HS)
#define RW(u, hs0, hs1) wa[u] = trd2(WRP((u) < 4 ? (hs0) : (hs1)) + ((u) & 3) * 2048, 512)
#define RX(c, u) xf[8 * (c) + (u)] = *(LAS bf16x8*)(((c) < 2 ? xr[(u) & 3] : xs[(u) & 3]) + (4 * ((c) & 1) + ((u) >> 2)) * HS)
#define M1(acc, c, u, SWAP) do { acc = SWAP ? MFMA(xf[8 * (c) + (u)], wa[u], acc) : MFMA(wa[u], xf[8 * (c) + (u)], acc); PIN(acc); } while (0)
#define G1(acc, c, u, SWAP, hs0, hs1, EXTRA) do { M1(acc, c, u, SWAP); RW(u, hs0, hs1); EXTRA; SB(); } while (0)
#define G0(acc, c, u, SWAP, EXTRA) do { M1(acc, c, u, SWAP); EXTRA; SB(); } while (0)
#define STEPX(acc, c, hs0, hs1) do { G1(acc, c, 0, false, hs0, hs1, RX((c) + 1, 0)); G1(acc, c, 1, false, hs0, hs1, RX((c) + 1, 1)); G1(acc, c, 2, false, hs0, hs1, RX((c) + 1, 2)); G1(acc, c, 3, false, hs0, hs1, RX((c) + 1, 3)); \
        G1(acc, c, 4, false, hs0, hs1, RX((c) + 1, 4)); G1(acc, c, 5, false, hs0, hs1, RX((c) + 1, 5)); G1(acc, c, 6, false, hs0, hs1, RX((c) + 1, 6)); G1(acc, c, 7, false, hs0, hs1, RX((c) + 1, 7)); } while (0)
#define STEP(acc, c, SWAP, hs0, hs1, E0, E1, E2, E3, E4, E5, E6, E7) do { G1(acc, c, 0, SWAP, hs0, hs1, E0); G1(acc, c, 1, SWAP, hs0, hs1, E1); G1(acc, c, 2, SWAP, hs0, hs1, E2); G1(acc, c, 3, SWAP, hs0, hs1, E3); \
        G1(acc, c, 4, SWAP, hs0, hs1, E4); G1(acc, c, 5, SWAP, hs0, hs1, E5); G1(acc, c, 6, SWAP, hs0, hs1, E6); G1(acc, c, 7, SWAP, hs0, hs1, E7); } while (0)
        BARX();
#pragma unroll
        for (int u = 0; u < 8; ++u) { RX(0, u); RW(u, 2, 3); }
        BARX();
        STEPX(accA, 0, 6, 7);
        BARX();
        STEPX(accA, 1, 10, 11);
        BARX();
        STEPX(accA, 2, 14, 15);
        BARX();
        STEP(accA, 3, false, 16, 18, , , , , , , , );
        BARX();
        u32x4 ka, cc;
        STEP(accB, 0, true, 19, 0, ,
             _Pragma("unroll") for (int i = 0; i < 4; ++i) ka[i] = pk(accA[2 * i], accA[2 * i + 1]);
             ST_SC1(ka, oP - 1024),
             _Pragma("unroll") for (int i = 0; i < 4; ++i) k4[i] = pk(accA[8 + 2 * i], accA[8 + 2 * i + 1]);
             ST_SC1(k4, oP),
             cc[0] = nonce - csum(ka, 0); PIN(cc[0]),
             cc[1] = nonce - csum(k4, 0); PIN(cc[1]),
             cc[2] = dppx1(cc[0]); cc[3] = dppx1(cc[1]); ST_SC1(cc, sC),
             __builtin_amdgcn_raw_buffer_store_b64(__builtin_bit_cast(u32x2, fval), rs, foff, 0, 16), );
        BARX();
        STEP(accB, 1, true, 1, 2, , , , , , , , );
        BARX();
        STEP(accB, 2, true, 3, 4, , , , , , , , );
        BARX();
        STEP(accB, 3, true, 17, 5, , , , , , , , );
        BARX();
        accA = f32x16{};
        u32x4 ra;
        STEP(accA, 0, false, 6, 7, ,
             _Pragma("unroll") for (int i = 0; i < 4; ++i) ra[i] = pk(accB[2 * i], accB[2 * i + 1]); PIN(ra),
             _Pragma("unroll") for (int i = 0; i < 4; ++i) va4[i] = pk(accB[8 + 2 * i], accB[8 + 2 * i + 1]); PIN(va4),
             UNF(ra, va4),
             cc[0] = nonce - csum(ra, 0); PIN(cc[0]),
             cc[1] = nonce - csum(va4, 0); PIN(cc[1]),
             cc[2] = dppx1(cc[0]); cc[3] = dppx1(cc[1]); PIN(cc), );
        STEP(accA, 1, false, 8, 9,
             *(LAS u32x4*)(lds + L_H + d * 2048 + lane * 16) = ka, ,
             *(LAS u32x4*)(lds + L_H + d * 2048 + 1024 + lane * 16) = ra, ,
             vb4 = fill4(ONES),
             vb0 = fill4(i0 <= 0 ? ONES : 0u); vb1 = fill4(i0 <= 1 ? ONES : 0u),
             vb2 = fill4(i0 <= 2 ? ONES : 0u); vb3 = fill4(i0 <= 3 ? ONES : 0u), );
        STEP(accA, 2, false, 10, 11, ST_SC1(ra, oP - 1024 + (WS_V - WS_K)), , ST_SC1(va4, oP + (WS_V - WS_K)), , ST_SC1(cc, sC + (WS_V - WS_K)), , , );
#pragma unroll
        for (int u = 0; u < 8; ++u) M1(accA, 3, u, false);
        SB();
        {
            u32x4 t, tp;
#pragma unroll
            for (int i = 0; i < 4; ++i) { tp[i] = pk(accA[2 * i] * QSCALE, accA[2 * i + 1] * QSCALE); t[i] = pk(accA[8 + 2 * i] * QSCALE, accA[8 + 2 * i + 1] * QSCALE); }
            qfo = asbf(t);
            *(LAS u32x4*)(lds + L_Q + d * 2048 + lane * 16) = tp;
            *(LAS u32x4*)(lds + L_Q + d * 2048 + 1024 + lane * 16) = t;
        }
        WAIT_BARL(0);
        __builtin_amdgcn_s_setprio(0);
        k3 = *(LAS u32x4*)(lds + L_K + d * 4096 + 3072 + lane * 16);
        k0 = *(LAS u32x4*)(lds + L_K + d * 4096 + lane * 16);
        k1 = *(LAS u32x4*)(lds + L_K + d * 4096 + 1024 + lane * 16);
        k2 = *(LAS u32x4*)(lds + L_K + d * 4096 + 2048 + lane * 16);
        fn = *(LAS unsigned*)(lds + L_N + d * 256 + lane * 4);
        asm volatile("s_waitcnt vmcnt(7)" : "+v"(dummy) :: "memory");
    }
    qfx = *(LAS bf16x8*)(lds + L_Q + (d ^ 1) * 2048 + kh * 1024 + lane * 16);
    const f32x16 zero = {};
    const float NINF = -__builtin_inff();
    f32x16 sa, sb, nmo, nmx;
    sa = MFMA(asbf(k3), qfo, zero); PIN(sa);
    int rot_ = rot;
    const char* ob = wo; PINS(ob); ob += (size_t)(4 * w) * 1024 + cb * 256;
    const unsigned ldw = lds0 + (unsigned)w * 1024;
#define DMA_O(h) do { PINS(rot_); glds16(voffO, ob + (size_t)((((h) >> 1) + rot_) & 7) * 65536 + ((h) & 1) * 32768, ldw + WOS[h] * HS); } while (0)
    float mo, mx;
    f32x16 ox = {}, o = {};
    u32x4 p0, p1, d0, d1, a0, a1, b0, b1;
    sb = MFMA(asbf(k3), qfx, zero); PIN(sb);
    mo = hmerge(max16(sa, sa[0])) + 32.f;
    nmo = bcast2(-mo); PIN(nmo); SB();
    sa = MFMA(asbf(k3), qfo, nmo); PIN(sa);
    f32x16 sc = MFMA(asbf(k4), qfo, nmo), sd = MFMA(asbf(k0), qfo, nmo);
    PIN(sc); PIN(sd);
    mx = hmerge(max16(sb, sb[0])) + 32.f;
    nmx = bcast2(-mx); PIN(nmx); SB();
    sb = MFMA(asbf(k3), qfx, nmx); PIN(sb);
    LOAD_V();
    exp_tile(sa, a0, a1); PIN(a0); PIN(a1); SB();
    UNF(va4, vb4);
    const unsigned n0 = __builtin_amdgcn_readlane(fn, 3), n1 = __builtin_amdgcn_readlane(fn, 2), n2 = __builtin_amdgcn_readlane(fn, 1), n3 = __builtin_amdgcn_readlane(fn, 0);
    const unsigned nO = __builtin_amdgcn_readlane(fn, 4 + w);
    if (mt) { sa = MFMA(asbf(k0), qfx, nmx); PIN(sa); } else { sa = MFMA(asbf(k4), qfx, nmx); PIN(sa); FENCE(); }
    DMA_O(7); DMA_O(8);
#pragma unroll
    for (int i = 0; i < 16; ++i) sc[i] = __builtin_amdgcn_inverse_ballot_w64(diag_mask(i)) ? sc[i] : sd[i];
    exp_tile(sc, p0, p1);
#pragma unroll
    for (int i = 0; i < 4; ++i) { d0[i] = p0[i] & dm0[i]; d1[i] = p1[i] & dm1[i]; p0[i] ^= d0[i]; p1[i] ^= d1[i]; }
    PIN(p0); PIN(p1); PIN(d0); PIN(d1); SB();
    PV(o, va4, vb4, d0, d1); PIN(o);
    DMA_O(9); DMA_O(10);
    exp_tile(sb, b0, b1); PIN(b0); PIN(b1); SB();
    sb = MFMA(asbf(k1), qfx, nmx); PIN(sb);
    DMA_O(11); DMA_O(12);
    exp_tile(sa, d0, d1); PIN(d0); PIN(d1); SB();
    WAIT_V(6);
    CSUM4(cv0, cv1, cv2, cv3, va0, va1, va2, va3);
    if (__builtin_expect(__any((cv0 != n0) | (cv1 != n1) | (cv2 != n2) | (cv3 != n3)), 0)) {
#pragma unroll 1
        for (int spins = 0; spins < MAXSPIN; ++spins) {
            __builtin_amdgcn_s_sleep(2);
            LOAD_V(); WAIT_V(0);
            CSUM4(cv0, cv1, cv2, cv3, va0, va1, va2, va3);
            if (!__any((cv0 != n0) | (cv1 != n1) | (cv2 != n2) | (cv3 != n3))) break;
        }
    }
    UNF(va3, vb3); UNF(va0, vb0);
    SB();
    const lptr exw = lds + (kh ? L_K + d * 4096 : L_H + d * 2048) + lane * 16;
    const lptr eyw = lds + L_Y + w * 512 + lane * 8;
    if (mt) { PV(ox, va0, vb0, d0, d1); PIN(ox); } else { PV(ox, va4, vb4, d0, d1); PIN(ox); FENCE(); }
    PV(o, va3, vb3, a0, a1); PIN(o); DMA_O(13); DMA_O(14);
    UNF(va1, vb1); UNF(va2, vb2); SB();
    sa = MFMA(asbf(k2), qfx, nmx); PIN(sa);
    PV(ox, va3, vb3, b0, b1); PIN(ox); PV(o, va0, vb0, p0, p1); PIN(o);
    exp_tile(sb, b0, b1); PIN(b0); PIN(b1); SB();
    sb = MFMA(asbf(k1), qfo, nmo); PIN(sb);
    PV(ox, va1, vb1, b0, b1); PIN(ox);
    exp_tile(sa, a0, a1); PIN(a0); PIN(a1); SB();
    sa = MFMA(asbf(k2), qfo, nmo); PIN(sa);
    PV(ox, va2, vb2, a0, a1); PIN(ox);
    exp_tile(sb, b0, b1); PIN(b0); PIN(b1); SB();
    PV(o, va1, vb1, b0, b1); PIN(o);
    *(LAS f32x4*)(exw) = f32x4{ox[0], ox[1], ox[2], ox[3]};
    *(LAS f32x4*)(exw + 1024) = f32x4{ox[4], ox[5], ox[6], ox[7]};
    *(LAS f32x2*)(eyw) = f32x2{ox[8], mx};
    u64 bad = part_bad(ox);
    if (b == 0) bad = 1;
    PINS(bad);
    SB();
    exp_tile(sa, a0, a1);
    PV(o, va2, vb2, a0, a1);
    bad |= part_bad(o);
    if (__builtin_expect(bad != 0, 0)) {
        u32x4 kx, vxa, vxb;
#pragma unroll
        for (int i = 0; i < 4; ++i) { kx[i] = mt ? k0[i] : k4[i]; vxa[i] = mt ? va0[i] : va4[i]; vxb[i] = mt ? vb0[i] : vb4[i]; }
        sc = MFMA(asbf(k4), qfo, zero);
        if (i0 == 0) sd = MFMA(asbf(k0), qfo, zero); else sd = bcast(NINF);
#pragma unroll
        for (int i = 0; i < 16; ++i) sc[i] = __builtin_amdgcn_inverse_ballot_w64(diag_mask(i)) ? sc[i] : sd[i];
        mo = max16(sc, NINF); mx = NINF;
        const bool hx = mt == 0 || i0 == 0;
        if (hx) { const f32x16 s = MFMA(asbf(kx), qfx, zero); mx = max16(s, mx); }
        if (i0 <= 1) { const f32x16 s1 = MFMA(asbf(k1), qfo, zero), s2 = MFMA(asbf(k1), qfx, zero); mo = max16(s1, mo); mx = max16(s2, mx); }
        if (i0 <= 2) { const f32x16 s1 = MFMA(asbf(k2), qfo, zero), s2 = MFMA(asbf(k2), qfx, zero); mo = max16(s1, mo); mx = max16(s2, mx); }
        if (i0 <= 3) { const f32x16 s1 = MFMA(asbf(k3), qfo, zero), s2 = MFMA(asbf(k3), qfx, zero); mo = max16(s1, mo); mx = max16(s2, mx); }
        mo = hmerge(mo); mx = hmerge(mx);
        nmo = bcast(-mo); nmx = bcast(-mx);
        o = zero; ox = zero;
#pragma unroll
        for (int i = 0; i < 16; ++i) sc[i] -= mo;
        exp_tile(sc, p0, p1);
#pragma unroll
        for (int i = 0; i < 4; ++i) { d0[i] = p0[i] & dm0[i]; d1[i] = p1[i] & dm1[i]; p0[i] ^= d0[i]; p1[i] ^= d1[i]; }
        PV(o, va4, vb4, d0, d1);
        if (i0 == 0) PV(o, va0, vb0, p0, p1);
        if (hx) { const f32x16 s = MFMA(asbf(kx), qfx, nmx); exp_tile(s, a0, a1); PV(ox, vxa, vxb, a0, a1); }
        if (i0 <= 1) { const f32x16 s = MFMA(asbf(k1), qfx, nmx), t = MFMA(asbf(k1), qfo, nmo); exp_tile(s, a0, a1); PV(ox, va1, vb1, a0, a1); exp_tile(t, a0, a1); PV(o, va1, vb1, a0, a1); }
        if (i0 <= 2) { const f32x16 s = MFMA(asbf(k2), qfx, nmx), t = MFMA(asbf(k2), qfo, nmo); exp_tile(s, a0, a1); PV(ox, va2, vb2, a0, a1); exp_tile(t, a0, a1); PV(o, va2, vb2, a0, a1); }
        if (i0 <= 3) { const f32x16 s = MFMA(asbf(k3), qfx, nmx), t = MFMA(asbf(k3), qfo, nmo); exp_tile(s, a0, a1); PV(ox, va3, vb3, a0, a1); exp_tile(t, a0, a1); PV(o, va3, vb3, a0, a1); }
        *(LAS f32x4*)(exw) = f32x4{ox[0], ox[1], ox[2], ox[3]};
        *(LAS f32x4*)(exw + 1024) = f32x4{ox[4], ox[5], ox[6], ox[7]};
        *(LAS f32x2*)(eyw) = f32x2{ox[8], mx};
    }
    BAR();
    {
        const lptr s = lds + (kh ? L_K + (d ^ 1) * 4096 : L_H + (d ^ 1) * 2048) + lane * 16;
        const f32x2 lm = *(LAS f32x2*)(lds + L_Y + (w ^ 1) * 512 + lane * 8);
        const f32x4 a = *(LAS f32x4*)(s), c = *(LAS f32x4*)(s + 1024);
        SB();
        const float M = __builtin_fmaxf(mo, lm[1]);
        float fo = __builtin_amdgcn_exp2f(mo - M), fp = __builtin_amdgcn_exp2f(lm[1] - M);
        const float inv = __builtin_amdgcn_rcpf(o[8] * fo + lm[0] * fp);
        fo *= inv; fp *= inv;
        u32x4 t, cc;
        t[0] = pk(o[0] * fo + a[0] * fp, o[1] * fo + a[1] * fp); t[1] = pk(o[2] * fo + a[2] * fp, o[3] * fo + a[3] * fp);
        t[2] = pk(o[4] * fo + c[0] * fp, o[5] * fo + c[1] * fp); t[3] = pk(o[6] * fo + c[2] * fp, o[7] * fo + c[3] * fp);
        ST_SC1(t, recO + lane * 16);
        cc[0] = nonce - csum(t, 0);
        cc[1] = __builtin_amdgcn_update_dpp(0, cc[0], 0x55, 0xF, 0xF, true); cc[2] = __builtin_amdgcn_update_dpp(0, cc[0], 0xAA, 0xF, 0xF, true); cc[3] = __builtin_amdgcn_update_dpp(0, cc[0], 0xFF, 0xF, 0xF, true);
        ST_SC1(cc, (lane & 3) ? WS_OOB : recO + 1024 + lane * 4);
    }
    WAIT_BAR(2);
    DMA_O(15);
    const int rh = w & 1, cq = w >> 1;
    bf16x8 wf0[16], wf1[16];
    lptr e0, f0;
    {
        const int rl = 16 * (g4 >> 1) + 4 * (g4 & 1) + q4;
        e0 = lds + rl * 256 + 32 * ((2 * cq) ^ (rl & 7)) + 8 * p4; lptr e1 = e0 + 8 * HS, e2 = e0 + 16 * HS;
        f0 = lds + rl * 256 + 32 * ((2 * cq + 1) ^ (rl & 7)) + 8 * p4; lptr f1 = f0 + 8 * HS, f2 = f0 + 16 * HS;
        PIN(e1); PIN(e2); PIN(f1); PIN(f2);
#pragma unroll
        for (int t = 0; t < 15; ++t) {
            wf0[t] = trd2((WOS[t] < 8 ? e0 : (WOS[t] < 16 ? e1 : e2)) + (WOS[t] & 7) * HS, 2048);
            wf1[t] = trd2((WOS[t] < 8 ? f0 : (WOS[t] < 16 ? f1 : f2)) + (WOS[t] & 7) * HS, 2048);
        }
    }
    SB();
    {
        const unsigned rO = WS_O + (unsigned)(qb3 * 32 + 4 * w) * REC + lane * 16, dC = 1024u - 12u * lane;
        u32x4 f0, f1, f2, f3; unsigned c0, c1, c2, c3;
        __builtin_amdgcn_s_sleep(OSLEEP);
#pragma unroll 1
        for (int spins = 0; spins < MAXSPIN; ++spins) {
            ALD1(c0, rO + dC); ALD4(f0, rO);
            ALD1(c1, rO + REC + dC); ALD4(f1, rO + REC);
            ALD1(c2, rO + 2 * REC + dC); ALD4(f2, rO + 2 * REC);
            ALD1(c3, rO + 3 * REC + dC); ALD4(f3, rO + 3 * REC);
            asm volatile("s_waitcnt vmcnt(0)" : "+v"(f0), "+v"(c0), "+v"(f1), "+v"(c1), "+v"(f2), "+v"(c2), "+v"(f3), "+v"(c3) :: "memory");
            CSUM4(c0, c1, c2, c3, f0, f1, f2, f3);
            if (!__any((c0 != nO) | (c1 != nO) | (c2 != nO) | (c3 != nO))) break;
            __builtin_amdgcn_s_sleep(2);
        }
        const lptr dd = lds + L_E + (4 * ((w - rot) & 7)) * 1024 + lane * 16;
        *(LAS u32x4*)(dd) = f0; *(LAS u32x4*)(dd + 1024) = f1; *(LAS u32x4*)(dd + 2048) = f2; *(LAS u32x4*)(dd + 3072) = f3;
    }
    BAR();
    wf0[15] = trd2(e0 + WOS[15] * HS, 2048); wf1[15] = trd2(f0 + WOS[15] * HS, 2048);
    f32x4 ac0 = {}, ac1 = {};
#define FOLD(dst, SC) do { const unsigned a0_ = pk(ac0[0] * (SC), ac0[1] * (SC)), a1_ = pk(ac0[2] * (SC), ac0[3] * (SC)), b0_ = pk(ac1[0] * (SC), ac1[1] * (SC)), b1_ = pk(ac1[2] * (SC), ac1[3] * (SC)); \
        auto t0_ = __builtin_amdgcn_permlane16_swap(a0_, b0_, false, false); auto t1_ = __builtin_amdgcn_permlane16_swap(a1_, b1_, false, false); \
        dst[0] = t0_[0]; dst[1] = t1_[0]; dst[2] = t0_[1]; dst[3] = t1_[1]; } while (0)
    {
        const lptr ob2 = lds + L_E + (g4 >> 1) * 1024 + (16 * rh + c16 + 32 * (g4 & 1)) * 16;
#pragma unroll
        for (int t = 0; t < 16; ++t) {
            const bf16x8 a = *(LAS bf16x8*)(ob2 + t * 2048);
            ac0 = MFMA16(wf0[t], a, ac0); ac1 = MFMA16(wf1[t], a, ac1);
        }
        u32x4 t;
        FOLD(t, 1.0f);
        char* po = out + (size_t)(32 * qb3 + 16 * rh + c16) * 1024 + (128 * cb + 32 * cq + 16 * (g4 & 1) + 8 * (g4 >> 1)) * 2;
        asm volatile("global_store_dwordx4 %0, %1, off sc1" :: "v"(po), "v"(t) : "memory");
    }
    if (tid == 0) {
        u64* pg = (u64*)(ws + WS_G + self * 128);
        const u64 val = hsh(gself, 1000);
        asm volatile("global_store_dwordx2 %0, %1, off sc1" :: "v"(pg), "v"(val) : "memory");
    }
}

extern "C" void kernel_launch(void* const* d_in, const int* in_sizes, int n_in,
                              void* d_out, int out_size, void* d_ws, size_t ws_size,
                              hipStream_t stream) {
    (void)hipFuncSetAttribute((const void*)fused_kernel, hipFuncAttributeMaxDynamicSharedMemorySize, LDS_BYTES);
    fused_kernel<<<256, 512, LDS_BYTES, stream>>>((const char*)d_in[0], (const char*)d_in[1], (const char*)d_in[2], (char*)d_out, (char*)d_ws);
}
```

```cpp
#include <hip/hip_runtime.h>
#include <hip/hip_bf16.h>
#include <stdint.h>

typedef __attribute__((ext_vector_type(8))) short bf16x8;
typedef __attribute__((ext_vector_type(4))) short s16x4;
typedef __attribute__((ext_vector_type(16))) float f32x16;
typedef __attribute__((ext_vector_type(4))) float f32x4;
typedef __attribute__((ext_vector_type(2))) float f32x2;
typedef __attribute__((ext_vector_type(4))) unsigned u32x4;
typedef __attribute__((ext_vector_type(2))) unsigned u32x2;
typedef __attribute__((ext_vector_type(2))) __bf16 bf16x2;
typedef __attribute__((ext_vector_type(2))) unsigned short u16x2;
typedef unsigned long long u64;
#define LAS __attribute__((address_space(3)))
#define DI __device__ __forceinline__
typedef LAS char* lptr;

constexpr unsigned REC2 = 2560, REC = 1280;
constexpr unsigned WS_K = 0, WS_V = 1024 * REC2, WS_O = 2048 * REC2, WS_G = WS_O + 2048 * REC, WS_F = WS_G + 256 * 128, WS_END = WS_F + 256 * 128;
constexpr unsigned WS_OOB = 0x7FFFF000u;
constexpr unsigned HS = 8192;
constexpr unsigned L_Q = 8 * HS, L_H = 9 * HS;
constexpr unsigned L_X = 9 * HS, L_Y = 11 * HS;
constexpr unsigned L_E = 8 * HS;
constexpr unsigned LDS_BYTES = 20 * HS;
#define KSLEEP 2
#define OSLEEP 2
#define CPF 4
constexpr unsigned CODE_BYTES = 21 * 1024;
constexpr int MAXSPIN = 1 << 15;
constexpr float QSCALE = 0.25f * 1.4426950408889634f;
constexpr unsigned ONES = 0x3F803F80u;
constexpr int WOS[16] = {0, 1, 2, 3, 4, 5, 6, 7, 12, 13, 14, 15, 16, 17, 18, 19};

#define MFMA(a, b, c) __builtin_amdgcn_mfma_f32_32x32x16_bf16((a), (b), (c), 0, 0, 0)
#define MFMA16(a, b, c) __builtin_amdgcn_mfma_f32_16x16x32_bf16((a), (b), (c), 0, 0, 0)
#define SB() __builtin_amdgcn_sched_barrier(0)
#define PIN(x) asm volatile("" : "+v"(x))
#define PINS(x) asm volatile("" : "+s"(x))
#define FENCE() asm volatile("" ::: "memory")
constexpr int wcnt(int vm, int lgkm) { return (vm & 15) | (7 << 4) | (lgkm << 8) | ((vm >> 4) << 14); }
#define WAIT_BAR(N) do { FENCE(); __builtin_amdgcn_s_waitcnt(wcnt(N, 0)); __builtin_amdgcn_s_barrier(); FENCE(); SB(); } while (0)
#define WAIT_BARV(N) do { FENCE(); __builtin_amdgcn_s_waitcnt(wcnt(N, 15)); __builtin_amdgcn_s_barrier(); FENCE(); SB(); } while (0)
#define WAIT_BARL(L) do { FENCE(); __builtin_amdgcn_s_waitcnt(wcnt(63, L)); __builtin_amdgcn_s_barrier(); FENCE(); SB(); } while (0)
#define BARX() do { FENCE(); __builtin_amdgcn_s_barrier(); FENCE(); SB(); } while (0)
#define BAR() WAIT_BAR(63)
#define MX3(a, b, c) __builtin_fmaxf(__builtin_fmaxf((a), (b)), (c))
#define ST_SC1(v, off) __builtin_amdgcn_raw_buffer_store_b128((v), rs, (off), 0, 16)
#define ST1_SC1(v, off) __builtin_amdgcn_raw_buffer_store_b32((v), rs, (off), 0, 16)
#define ALD4(dst, off) asm volatile("buffer_load_dwordx4 %0, %1, %2, 0 offen sc1" : "=v"(dst) : "v"(off), "s"(rs) : "memory")
#define ALD1(dst, off) asm volatile("buffer_load_dword %0, %1, %2, 0 offen sc1" : "=v"(dst) : "v"(off), "s"(rs) : "memory")
#define ALD4S(dst, off, so) asm volatile("buffer_load_dwordx4 %0, %1, %2, %3 offen sc1" : "=v"(dst) : "v"(off), "s"(rs), "s"(so) : "memory")
#define ALD1S(dst, off, so) asm volatile("buffer_load_dword %0, %1, %2, %3 offen sc1" : "=v"(dst) : "v"(off), "s"(rs), "s"(so) : "memory")

#define KATTR

DI unsigned pk(float a, float b) { f32x2 v = {a, b}; bf16x2 r = __builtin_convertvector(v, bf16x2); return __builtin_bit_cast(unsigned, r); }
DI void glds16(unsigned voff, const char* sbase, unsigned lds_dst) {
    asm volatile("s_mov_b32 m0, %2\n\ts_nop 0\n\tglobal_load_lds_dwordx4 %0, %1" :: "v"(voff), "s"(sbase), "s"(lds_dst) : "memory");
}
DI s16x4 trd(lptr p) { return __builtin_amdgcn_ds_read_tr16_b64_v4i16((LAS s16x4*)p); }
DI bf16x8 trd2(lptr p, int off2) { s16x4 a = trd(p), b = trd(p + off2); return __builtin_shufflevector(a, b, 0, 1, 2, 3, 4, 5, 6, 7); }
DI u64 hsh(u64 g, unsigned slot) { return g * 0x9E3779B97F4A7C15ull + (u64)(2u * slot + 1u) * 0xD1B54A32D192ED03ull; }
DI bf16x8 asbf(u32x4 v) { return __builtin_bit_cast(bf16x8, v); }
DI unsigned d2(unsigned a, unsigned k, unsigned c) { return __builtin_amdgcn_udot2(__builtin_bit_cast(u16x2, a), __builtin_bit_cast(u16x2, k), c, false); }
DI unsigned csum(u32x4 v, unsigned c) { return d2(v.x, 0x682f978bu, d2(v.y, 0x92e1a2d1u, d2(v.z, 0x4ca14349u, d2(v.w, 0xf6314f97u, c)))); }
DI float max16(const f32x16& s, float m) {
#pragma unroll
    for (int i = 0; i < 16; i += 2) m = MX3(m, s[i], s[i + 1]);
    return m;
}
DI float hmerge(float m) { auto rr = __builtin_amdgcn_permlane32_swap(__float_as_uint(m), __float_as_uint(m), false, false);
    return __builtin_fmaxf(__uint_as_float(rr[0]), __uint_as_float(rr[1])); }
constexpr int crow(int r, int hi) { return (r & 3) + 8 * (r >> 2) + 4 * hi; }
constexpr u64 diag_mask(int reg) { u64 m = 0; for (int l = 0; l < 64; ++l) if (crow(reg, l >> 5) <= (l & 31)) m |= 1ull << l; return m; }
DI f32x16 bcast(float v) { f32x16 r;
#pragma unroll
    for (int i = 0; i < 16; ++i) r[i] = v;
    return r; }
DI f32x16 bcast2(float v) {
    const f32x2 p = {v, v}; f32x2 q[8]; q[0] = p;
#pragma unroll
    for (int i = 1; i < 8; ++i) asm volatile("v_mov_b64 %0, %1" : "=v"(q[i]) : "v"(p));
    f32x16 r;
#pragma unroll
    for (int i = 0; i < 16; ++i) r[i] = q[i >> 1][i & 1];
    return r; }
DI unsigned dppx1(unsigned v) { return __builtin_amdgcn_update_dpp(0, v, 0xB1, 0xF, 0xF, true); }
DI u32x4 fill4(unsigned v) {
    const u64 p = ((u64)v << 32) | v; u32x2 a, b;
    asm volatile("v_mov_b64 %0, %1" : "=v"(a) : "s"(p)); asm volatile("v_mov_b64 %0, %1" : "=v"(b) : "s"(p));
    return u32x4{a[0], a[1], b[0], b[1]}; }
#define CS5(c, K) do { e0 = d2(v0.c, K, e0); e1 = d2(v1.c, K, e1); e2 = d2(v2.c, K, e2); e3 = d2(v3.c, K, e3); e4 = d2(v4.c, K, e4); asm volatile("" : "+v"(e0), "+v"(e1), "+v"(e2), "+v"(e3), "+v"(e4)); } while (0)
DI void csum5(unsigned& e0, unsigned& e1, unsigned& e2, unsigned& e3, unsigned& e4, const u32x4& v0, const u32x4& v1, const u32x4& v2, const u32x4& v3, const u32x4& v4) {
    CS5(w, 0xf6314f97u); CS5(z, 0x4ca14349u); CS5(y, 0x92e1a2d1u); CS5(x, 0x682f978bu); }
#define CSUM4(e0, e1, e2, e3, v0, v1, v2, v3) do { \
    e0 = d2(v0.w, 0xf6314f97u, e0); e1 = d2(v1.w, 0xf6314f97u, e1); e2 = d2(v2.w, 0xf6314f97u, e2); e3 = d2(v3.w, 0xf6314f97u, e3); asm volatile("" : "+v"(e0), "+v"(e1), "+v"(e2), "+v"(e3)); \
    e0 = d2(v0.z, 0x4ca14349u, e0); e1 = d2(v1.z, 0x4ca14349u, e1); e2 = d2(v2.z, 0x4ca14349u, e2); e3 = d2(v3.z, 0x4ca14349u, e3); asm volatile("" : "+v"(e0), "+v"(e1), "+v"(e2), "+v"(e3)); \
    e0 = d2(v0.y, 0x92e1a2d1u, e0); e1 = d2(v1.y, 0x92e1a2d1u, e1); e2 = d2(v2.y, 0x92e1a2d1u, e2); e3 = d2(v3.y, 0x92e1a2d1u, e3); asm volatile("" : "+v"(e0), "+v"(e1), "+v"(e2), "+v"(e3)); \
    e0 = d2(v0.x, 0x682f978bu, e0); e1 = d2(v1.x, 0x682f978bu, e1); e2 = d2(v2.x, 0x682f978bu, e2); e3 = d2(v3.x, 0x682f978bu, e3); asm volatile("" : "+v"(e0), "+v"(e1), "+v"(e2), "+v"(e3)); } while (0)

DI void exp_tile(const f32x16& s, u32x4& p0, u32x4& p1) {
#pragma unroll
    for (int i = 0; i < 4; ++i) p0[i] = pk(__builtin_amdgcn_exp2f(s[2 * i]), __builtin_amdgcn_exp2f(s[2 * i + 1]));
#pragma unroll
    for (int i = 0; i < 4; ++i) p1[i] = pk(__builtin_amdgcn_exp2f(s[8 + 2 * i]), __builtin_amdgcn_exp2f(s[8 + 2 * i + 1]));
}
#define PV(o, va, vb, p0, p1) do { o = MFMA(asbf(va), asbf(p0), o); o = MFMA(asbf(vb), asbf(p1), o); } while (0)
DI bool part_bad(const f32x16& o) {
    const float z = ((o[0] + o[1]) + (o[2] + o[3])) + ((o[4] + o[5]) + (o[6] + o[7]));
    return !(o[8] < 0x1p120f) || !(__builtin_fabsf(z) < __builtin_inff());
}

__global__ void __launch_bounds__(512, 2) KATTR
fused_kernel(const char* __restrict__ x, const char* __restrict__ wqkv, const char* __restrict__ wo, char* __restrict__ out, char* ws) {
    extern __shared__ __attribute__((aligned(16))) char smem[];
    const lptr lds = (lptr)smem;
    const unsigned lds0 = (unsigned)(uintptr_t)smem;
    const char* pc0;
    asm volatile("s_getpc_b64 %0" : "=s"(pc0));
    const int tid = threadIdx.x, lane = tid & 63;
    const int w = __builtin_amdgcn_readfirstlane(tid >> 6);
    const int id = blockIdx.x, xcd = id & 7, idx = id >> 3;
    const int b = 8 * (xcd >> 1) + (idx >> 2), g = 4 * (xcd & 1) + (idx & 3);
    const int rho0 = ((idx >> 2) + 2 * (idx & 3)) & 7;
    const int d = w & 3;
    const unsigned voffX = (unsigned)(lane >> 3) * 1024u + (unsigned)((lane & 7) ^ ((4 * d + (lane >> 4)) & 7)) * 16u;
    const unsigned voffW = (unsigned)(lane >> 3) * 3072u + (unsigned)((lane & 7) ^ (((lane >> 4) & 1) << 2)) * 16u;
    const unsigned ldd = lds0 + (unsigned)d * 1024;
    asm volatile("" :: "s"(x), "s"(wqkv), "s"(wo), "s"(out), "s"(ws));
    const char* xb = x + (size_t)(64 * b + 8 * d) * 1024;
    const char* wb = wqkv + (size_t)(8 * d) * 3072 + g * 128;
#define SX(p) (4 * ((p) >> 1) + ((p) & 1))
#define SK(p) (4 * ((p) >> 1) + 2 + ((p) & 1))
#define DMA_X(p) do { PINS(rho); const char* s_ = xb + (((p) + rho) & 7) * 128; glds16(voffX, s_, ldd + SX(p) * HS); glds16(voffX, s_ + 32768, ldd + SX(p) * HS + 4096); } while (0)
#define DMA_W(matoff, p, hs) do { PINS(rho); const char* s_ = wb + (size_t)(((p) + rho) & 7) * (64 * 3072) + (matoff); glds16(voffW, s_, ldd + (hs) * HS); glds16(voffW, s_ + 32 * 3072, ldd + (hs) * HS + 4096); } while (0)
#define DMA_K(p) DMA_W(1024, p, SK(p))
#define DMA_V(p, hs) DMA_W(2048, p, hs)
#define DMA_Q(p, hs) DMA_W(0, p, hs)
    if (w < 4) {
        int rho = rho0;
        DMA_X(0); DMA_K(0); DMA_X(1); DMA_K(1); DMA_V(0, 16); DMA_Q(0, 17);
    }
    SB();
    const int r = lane & 31, hh = lane >> 5;
    const int mt = w & 1, j = (w >> 1) & 1, kh = w >> 2;
    const int head = 4 * g + 2 * j + kh;
    const int qo = 2 * b + mt;
    const int self = b * 8 + g;
    const int i0 = b < 4 ? 4 - b : 0;
    const __amdgpu_buffer_rsrc_t rs = __builtin_amdgcn_make_buffer_rsrc(ws, 0, WS_END, 0x00020000);
    const unsigned recK = WS_K + (unsigned)((2 * g + j) * 64 + qo) * REC2;
    const unsigned recO = WS_O + (unsigned)(qo * 32 + head) * REC;
    const int cb = g & 3, qb3 = 2 * b + (g >> 2), rot = idx >> 2;
    const int cons = lane < 4 ? (b + 1 + lane) * 8 + g : b * 8 + ((lane - 4) & 7);
    const bool cons_ok = lane < 4 ? (b + 1 + lane < 32) : (lane < 12);
    const unsigned cslot = lane < 4 ? lane : 4 + g;
    u64 gcons;
    {
        const char* pgc = ws + WS_G + (cons_ok ? cons : self) * 128;
        asm volatile("global_load_dwordx2 %0, %1, off sc1" : "=v"(gcons) : "v"(pgc) : "memory");
    }
    unsigned dummy = 0;
#define CW0 0
    const int c16 = lane & 15, g4 = lane >> 4, q4 = c16 >> 2, p4 = lane & 3;
    u32x4 k0, k1, k2, k3, k4, va0, va1, va2, va3, va4, vb0, vb1, vb2, vb3, vb4;
    unsigned ck0, ck1, ck2, ck3, cv0, cv1, cv2, cv3;
    u64 fw;
    bf16x8 qfo, qfx;
    const char* pfs = ws + WS_F + (unsigned)self * 128 + (lane < 12 ? lane : 0) * 8;
    const bool needF = lane < 4 ? (b - 1 - lane >= 0) : (lane < 12);
    const unsigned oP = recK + kh * 1024 + lane * 16, oC = recK + 2048 + lane * 8 + kh * 4;
    const unsigned oK0 = i0 <= 0 ? oP - 8 * REC2 : WS_OOB, oK1 = i0 <= 1 ? oP - 6 * REC2 : WS_OOB, oK2 = i0 <= 2 ? oP - 4 * REC2 : WS_OOB, oK3 = i0 <= 3 ? oP - 2 * REC2 : WS_OOB;
    const unsigned cK0 = i0 <= 0 ? oC - 8 * REC2 : WS_OOB, cK1 = i0 <= 1 ? oC - 6 * REC2 : WS_OOB, cK2 = i0 <= 2 ? oC - 4 * REC2 : WS_OOB, cK3 = i0 <= 3 ? oC - 2 * REC2 : WS_OOB;
#define LOAD_K() do { ALD1(ck3, cK3); ALD4(k3, oK3); ALD1(ck0, cK0); ALD4(k0, oK0); ALD1(ck1, cK1); ALD4(k1, oK1); ALD1(ck2, cK2); ALD4(k2, oK2); } while (0)
#define LOAD_V() do { ALD1S(cv3, cK3, WS_V - WS_K); ALD4S(va3, oK3, WS_V - WS_K); ALD1S(cv0, cK0, WS_V - WS_K); ALD4S(va0, oK0, WS_V - WS_K); \
                      ALD1S(cv1, cK1, WS_V - WS_K); ALD4S(va1, oK1, WS_V - WS_K); ALD1S(cv2, cK2, WS_V - WS_K); ALD4S(va2, oK2, WS_V - WS_K); } while (0)
#define WAIT_K(N) asm volatile("s_waitcnt vmcnt(" #N ")" : "+v"(k0), "+v"(k1), "+v"(k2), "+v"(k3), "+v"(ck0), "+v"(ck1), "+v"(ck2), "+v"(ck3), "+v"(fw) :: "memory")
#define WAIT_V(N) asm volatile("s_waitcnt vmcnt(" #N ")" : "+v"(va0), "+v"(va1), "+v"(va2), "+v"(va3), "+v"(cv0), "+v"(cv1), "+v"(cv2), "+v"(cv3) :: "memory")
#define LOAD_F() asm volatile("global_load_dwordx2 %0, %1, off sc1" : "=v"(fw) : "v"(pfs) : "memory")
#define UNF(va, vb) do { _Pragma("unroll") for (int i = 0; i < 4; ++i) { auto t_ = __builtin_amdgcn_permlane16_swap(va[i], vb[i], false, false); va[i] = t_[0]; vb[i] = t_[1]; } PIN(va); PIN(vb); } while (0)
    u32x4 dm0, dm1;
#pragma unroll
    for (int i = 0; i < 4; ++i) {
        dm0[i] = (crow(2 * i, 0) + 4 * hh <= r ? 0xFFFFu : 0u) | (crow(2 * i + 1, 0) + 4 * hh <= r ? 0xFFFF0000u : 0u);
        dm1[i] = (crow(8 + 2 * i, 0) + 4 * hh <= r ? 0xFFFFu : 0u) | (crow(8 + 2 * i + 1, 0) + 4 * hh <= r ? 0xFFFF0000u : 0u);
    }
    PIN(dm0); PIN(dm1);
    if (w < 4) {
        int rho = rho0;
        DMA_X(2); DMA_K(2); DMA_X(3); DMA_K(3);
    }
    if (w < 4) __builtin_amdgcn_s_waitcnt(wcnt(8, 15)); else __builtin_amdgcn_s_waitcnt(wcnt(CW0, 15));
    FENCE();
    PIN(gcons);
    BARX();
    const u64 gself = ((u64)(unsigned)__builtin_amdgcn_readlane((unsigned)(gcons >> 32), 12) << 32) | (unsigned)__builtin_amdgcn_readlane((unsigned)gcons, 12);
    const unsigned nonce = (unsigned)(hsh(gself, 77) >> 32);
    unsigned wantF;
    if (w < 4) {
        int rho = rho0;
        DMA_X(4); DMA_K(4); DMA_X(5); DMA_K(5);
        __builtin_amdgcn_s_waitcnt(wcnt(8, 15));
        WAIT_BARV(8);
        DMA_X(6); DMA_K(6); DMA_X(7); DMA_K(7);
        WAIT_BARV(8);
        DMA_V(1, 18); DMA_V(2, 19); DMA_V(3, 0); DMA_V(4, 1);
        WAIT_BARV(8);
        DMA_V(5, 2); DMA_V(6, 3); DMA_V(7, 4);
        WAIT_BARV(12);
        DMA_Q(1, 5); DMA_Q(2, 6); DMA_Q(3, 7);
        WAIT_BARV(14);
        wantF = (unsigned)(hsh(gself, lane) >> 32); PIN(wantF);
        vb0 = fill4(i0 <= 0 ? ONES : 0u); vb1 = fill4(i0 <= 1 ? ONES : 0u); vb2 = fill4(i0 <= 2 ? ONES : 0u); vb3 = fill4(i0 <= 3 ? ONES : 0u); vb4 = fill4(ONES);
        __builtin_amdgcn_s_waitcnt(wcnt(10, 15));
        WAIT_BARV(10);
        DMA_Q(4, 12); DMA_Q(5, 13);
        WAIT_BARV(10);
        DMA_Q(6, 14); DMA_Q(7, 15);
        WAIT_BARV(12);
        WAIT_BARV(8);
        __builtin_amdgcn_s_waitcnt(wcnt(0, 15));
        WAIT_BARV(0);
        __builtin_amdgcn_s_sleep(KSLEEP);
        LOAD_F(); LOAD_K();
        BARX();
        qfo = *(LAS bf16x8*)(lds + L_Q + d * 2048 + lane * 16);
        k4 = *(LAS u32x4*)(lds + L_H + d * 2048 + lane * 16);
        va4 = *(LAS u32x4*)(lds + L_H + d * 2048 + 1024 + lane * 16);
        WAIT_K(0);
        UNF(va4, vb4);
    } else {
        __builtin_amdgcn_s_setprio(2);
        const int blk = (lane >> 4) & 1;
        lptr wr0 = lds + (8 * hh + q4) * 128 + 64 * (j ^ (q4 >> 1)) + 32 * blk + 8 * p4;
        lptr wr1 = wr0 + 8 * HS, wr2 = wr0 + 16 * HS;
        PIN(wr1); PIN(wr2);
        const int xrow = 32 * mt + r;
        lptr xr[4], xs[4];
#pragma unroll
        for (int i = 0; i < 4; ++i) { xr[i] = lds + xrow * 128 + (((2 * i + hh) ^ ((xrow >> 1) & 7)) * 16); xs[i] = xr[i] + 8 * HS; PIN(xs[i]); }
        bf16x8 xf[32], wa[8];
        f32x16 accA = {}, accB = {};
#define WRP(hs) (((hs) < 8 ? wr0 : ((hs) < 16 ? wr1 : wr2)) + ((hs) & 7) * HS)
#define RW(u, hs0, hs1) wa[u] = trd2(WRP((u) < 4 ? (hs0) : (hs1)) + ((u) & 3) * 2048, 512)
#define RX(c, u) xf[8 * (c) + (u)] = *(LAS bf16x8*)(((c) < 2 ? xr[(u) & 3] : xs[(u) & 3]) + (4 * ((c) & 1) + ((u) >> 2)) * HS)
#define M1(acc, c, u, SWAP) do { acc = SWAP ? MFMA(xf[8 * (c) + (u)], wa[u], acc) : MFMA(wa[u], xf[8 * (c) + (u)], acc); PIN(acc); } while (0)
#define G1(acc, c, u, SWAP, hs0, hs1, EXTRA) do { M1(acc, c, u, SWAP); RW(u, hs0, hs1); EXTRA; SB(); } while (0)
#define G0(acc, c, u, SWAP, EXTRA) do { M1(acc, c, u, SWAP); EXTRA; SB(); } while (0)
#define STEPX(acc, c, hs0, hs1) do { G1(acc, c, 0, false, hs0, hs1, RX((c) + 1, 0)); G1(acc, c, 1, false, hs0, hs1, RX((c) + 1, 1)); G1(acc, c, 2, false, hs0, hs1, RX((c) + 1, 2)); G1(acc, c, 3, false, hs0, hs1, RX((c) + 1, 3)); \
        G1(acc, c, 4, false, hs0, hs1, RX((c) + 1, 4)); G1(acc, c, 5, false, hs0, hs1, RX((c) + 1, 5)); G1(acc, c, 6, false, hs0, hs1, RX((c) + 1, 6)); G1(acc, c, 7, false, hs0, hs1, RX((c) + 1, 7)); } while (0)
#define STEP(acc, c, SWAP, hs0, hs1, E0, E1, E2, E3, E4, E5, E6, E7) do { G1(acc, c, 0, SWAP, hs0, hs1, E0); G1(acc, c, 1, SWAP, hs0, hs1, E1); G1(acc, c, 2, SWAP, hs0, hs1, E2); G1(acc, c, 3, SWAP, hs0, hs1, E3); \
        G1(acc, c, 4, SWAP, hs0, hs1, E4); G1(acc, c, 5, SWAP, hs0, hs1, E5); G1(acc, c, 6, SWAP, hs0, hs1, E6); G1(acc, c, 7, SWAP, hs0, hs1, E7); } while (0)
#pragma unroll
        for (int u = 0; u < 8; ++u) { RX(0, u); RW(u, 2, 3); }
        wantF = (unsigned)(hsh(gself, lane) >> 32); PIN(wantF);
        u64 fval = (hsh(gcons, cslot) & 0xFFFFFFFF00000000ull) | nonce; PIN(fval);
        unsigned foff = (w == 4 && cons_ok) ? WS_F + cons * 128 + cslot * 8 : WS_OOB; PIN(foff);
        unsigned sC = (lane & 1) ? WS_OOB : recK + 2048 + lane * 8; PIN(sC);
        BARX();
        STEPX(accA, 0, 6, 7);
        BARX();
        STEPX(accA, 1, 10, 11);
        BARX();
        STEPX(accA, 2, 14, 15);
        BARX();
        STEP(accA, 3, false, 16, 18, , , , , , , , );
        BARX();
        u32x4 ka, cc;
        STEP(accB, 0, true, 19, 0, ,
             _Pragma("unroll") for (int i = 0; i < 4; ++i) ka[i] = pk(accA[2 * i], accA[2 * i + 1]);
             ST_SC1(ka, oP - 1024),
             _Pragma("unroll") for (int i = 0; i < 4; ++i) k4[i] = pk(accA[8 + 2 * i], accA[8 + 2 * i + 1]);
             ST_SC1(k4, oP),
             cc[0] = nonce - csum(ka, 0); PIN(cc[0]),
             cc[1] = nonce - csum(k4, 0); PIN(cc[1]),
             cc[2] = dppx1(cc[0]); cc[3] = dppx1(cc[1]); ST_SC1(cc, sC),
             __builtin_amdgcn_raw_buffer_store_b64(__builtin_bit_cast(u32x2, fval), rs, foff, 0, 16), );
        BARX();
        STEP(accB, 1, true, 1, 2, , , , , , , , );
        BARX();
        STEP(accB, 2, true, 3, 4, , , , , , , , );
        BARX();
        STEP(accB, 3, true, 17, 5, , , , , , , , );
        BARX();
        accA = f32x16{};
        u32x4 ra;
        STEP(accA, 0, false, 6, 7, ,
             _Pragma("unroll") for (int i = 0; i < 4; ++i) ra[i] = pk(accB[2 * i], accB[2 * i + 1]); PIN(ra),
             _Pragma("unroll") for (int i = 0; i < 4; ++i) va4[i] = pk(accB[8 + 2 * i], accB[8 + 2 * i + 1]); PIN(va4),
             UNF(ra, va4); ST_SC1(ra, oP - 1024 + (WS_V - WS_K)),
             ST_SC1(va4, oP + (WS_V - WS_K)); cc[0] = nonce - csum(ra, 0); PIN(cc[0]),
             cc[1] = nonce - csum(va4, 0); PIN(cc[1]),
             cc[2] = dppx1(cc[0]); cc[3] = dppx1(cc[1]); ST_SC1(cc, sC + (WS_V - WS_K)), );
        BARX();
        STEP(accA, 1, false, 12, 13,
             *(LAS u32x4*)(lds + L_H + d * 2048 + lane * 16) = ka, ,
             *(LAS u32x4*)(lds + L_H + d * 2048 + 1024 + lane * 16) = ra, ,
             vb4 = fill4(ONES); UNF(va4, vb4),
             vb0 = fill4(i0 <= 0 ? ONES : 0u); vb1 = fill4(i0 <= 1 ? ONES : 0u),
             vb2 = fill4(i0 <= 2 ? ONES : 0u); vb3 = fill4(i0 <= 3 ? ONES : 0u), LOAD_F());
        STEP(accA, 2, false, 14, 15, ALD1(ck3, cK3), ALD4(k3, oK3), ALD1(ck0, cK0), ALD4(k0, oK0), ALD1(ck1, cK1), ALD4(k1, oK1), ALD1(ck2, cK2), ALD4(k2, oK2));
#pragma unroll
        for (int u = 0; u < 8; ++u) M1(accA, 3, u, false);
        SB();
        {
            u32x4 t, tp;
#pragma unroll
            for (int i = 0; i < 4; ++i) { tp[i] = pk(accA[2 * i] * QSCALE, accA[2 * i + 1] * QSCALE); t[i] = pk(accA[8 + 2 * i] * QSCALE, accA[8 + 2 * i + 1] * QSCALE); }
            qfo = asbf(t);
            *(LAS u32x4*)(lds + L_Q + d * 2048 + lane * 16) = tp;
            *(LAS u32x4*)(lds + L_Q + d * 2048 + 1024 + lane * 16) = t;
        }
        WAIT_BARL(0);
        __builtin_amdgcn_s_setprio(0);
        WAIT_K(0);
        PIN(dummy);
    }
    qfx = *(LAS bf16x8*)(lds + L_Q + (d ^ 1) * 2048 + kh * 1024 + lane * 16);
    if (__builtin_expect(__any(needF && (unsigned)(fw >> 32) != wantF), 0)) {
#pragma unroll 1
        for (int spins = 0; spins < MAXSPIN; ++spins) {
            asm volatile("global_load_dwordx2 %0, %1, off sc1\n\ts_waitcnt vmcnt(0)" : "=v"(fw) : "v"(pfs) : "memory");
            if (!__any(needF && (unsigned)(fw >> 32) != wantF)) break;
            __builtin_amdgcn_s_sleep(1);
        }
    }
    const unsigned f0_ = __builtin_amdgcn_readlane((unsigned)fw, 3), f1_ = __builtin_amdgcn_readlane((unsigned)fw, 2), f2_ = __builtin_amdgcn_readlane((unsigned)fw, 1), f3_ = __builtin_amdgcn_readlane((unsigned)fw, 0);
    const unsigned n0 = i0 <= 0 ? f0_ : 0u, n1 = i0 <= 1 ? f1_ : 0u, n2 = i0 <= 2 ? f2_ : 0u, n3 = i0 <= 3 ? f3_ : 0u;
    const unsigned nO = __builtin_amdgcn_readlane((unsigned)fw, 4 + w);
    const f32x16 zero = {};
    const float NINF = -__builtin_inff();
    f32x16 sa, sb, nmo, nmx;
    sa = MFMA(asbf(k3), qfo, zero); PIN(sa);
    CSUM4(ck0, ck1, ck2, ck3, k0, k1, k2, k3);
    if (__builtin_expect(__any((ck0 != n0) | (ck1 != n1) | (ck2 != n2) | (ck3 != n3)), 0)) {
#pragma unroll 1
        for (int spins = 0; spins < MAXSPIN; ++spins) {
            __builtin_amdgcn_s_sleep(2);
            LOAD_K(); WAIT_K(0);
            CSUM4(ck0, ck1, ck2, ck3, k0, k1, k2, k3);
            if (!__any((ck0 != n0) | (ck1 != n1) | (ck2 != n2) | (ck3 != n3))) break;
        }
        sa = MFMA(asbf(k3), qfo, zero); PIN(sa);
    }
    const unsigned voffO = (unsigned)(lane >> 4) * 1024u + (unsigned)((lane & 15) ^ ((4 * (w & 1) + (lane >> 4)) << 1)) * 16u;
    int rot_ = rot;
    const char* ob = wo; PINS(ob); ob += (size_t)(4 * w) * 1024 + cb * 256;
    const unsigned ldw = lds0 + (unsigned)w * 1024;
#define DMA_O(c) do { PINS(rot_); const char* s_ = ob + (size_t)(((c) + rot_) & 7) * 65536; \
        glds16(voffO, s_, ldw + WOS[2 * (c)] * HS); glds16(voffO, s_ + 32768, ldw + WOS[2 * (c) + 1] * HS); } while (0)
    float mo, mx;
    f32x16 ox = {}, o = {};
    u32x4 p0, p1, d0, d1, a0, a1, b0, b1;
    sb = MFMA(asbf(k3), qfx, zero); PIN(sb);
    mo = hmerge(max16(sa, sa[0])) + 32.f;
    nmo = bcast2(-mo); PIN(nmo); SB();
    sa = MFMA(asbf(k3), qfo, nmo); PIN(sa);
    f32x16 sc = MFMA(asbf(k4), qfo, nmo), sd = MFMA(asbf(k0), qfo, nmo);
    PIN(sc); PIN(sd);
    mx = hmerge(max16(sb, sb[0])) + 32.f;
    nmx = bcast2(-mx); PIN(nmx); SB();
    sb = MFMA(asbf(k3), qfx, nmx); PIN(sb);
    LOAD_V();
    exp_tile(sa, a0, a1); PIN(a0); PIN(a1); SB();
    if (mt) { sa = MFMA(asbf(k0), qfx, nmx); PIN(sa); } else { sa = MFMA(asbf(k4), qfx, nmx); PIN(sa); FENCE(); }
    DMA_O(0);
#pragma unroll
    for (int i = 0; i < 16; ++i) sc[i] = __builtin_amdgcn_inverse_ballot_w64(diag_mask(i)) ? sc[i] : sd[i];
    exp_tile(sc, p0, p1);
#pragma unroll
    for (int i = 0; i < 4; ++i) { d0[i] = p0[i] & dm0[i]; d1[i] = p1[i] & dm1[i]; p0[i] ^= d0[i]; p1[i] ^= d1[i]; }
    PIN(p0); PIN(p1); PIN(d0); PIN(d1); SB();
    PV(o, va4, vb4, d0, d1); PIN(o); DMA_O(1);
    exp_tile(sb, b0, b1); PIN(b0); PIN(b1); SB();
    sb = MFMA(asbf(k1), qfx, nmx); PIN(sb); DMA_O(2);
    exp_tile(sa, d0, d1); PIN(d0); PIN(d1); SB();
    WAIT_V(6);
    CSUM4(cv0, cv1, cv2, cv3, va0, va1, va2, va3);
    if (__builtin_expect(__any((cv0 != n0) | (cv1 != n1) | (cv2 != n2) | (cv3 != n3)), 0)) {
#pragma unroll 1
        for (int spins = 0; spins < MAXSPIN; ++spins) {
            __builtin_amdgcn_s_sleep(2);
            LOAD_V(); WAIT_V(0);
            CSUM4(cv0, cv1, cv2, cv3, va0, va1, va2, va3);
            if (!__any((cv0 != n0) | (cv1 != n1) | (cv2 != n2) | (cv3 != n3))) break;
        }
    }
    UNF(va3, vb3); UNF(va0, vb0);
    SB();
    const lptr exw = lds + L_X + w * 2048;
    if (mt) { PV(ox, va0, vb0, d0, d1); PIN(ox); } else { PV(ox, va4, vb4, d0, d1); PIN(ox); FENCE(); }
    PV(o, va3, vb3, a0, a1); PIN(o); DMA_O(3);
    UNF(va1, vb1); UNF(va2, vb2); SB();
    sa = MFMA(asbf(k2), qfx, nmx); PIN(sa);
    PV(ox, va3, vb3, b0, b1); PIN(ox); PV(o, va0, vb0, p0, p1); PIN(o); DMA_O(4);
    exp_tile(sb, b0, b1); PIN(b0); PIN(b1); SB();
    sb = MFMA(asbf(k1), qfo, nmo); PIN(sb);
    PV(ox, va1, vb1, b0, b1); PIN(ox); DMA_O(5);
    exp_tile(sa, a0, a1); PIN(a0); PIN(a1); SB();
    sa = MFMA(asbf(k2), qfo, nmo); PIN(sa);
    PV(ox, va2, vb2, a0, a1); PIN(ox); DMA_O(6); DMA_O(7);
    exp_tile(sb, b0, b1); PIN(b0); PIN(b1); SB();
    PV(o, va1, vb1, b0, b1); PIN(o);
    bool bad = part_bad(ox);
    SB();
    exp_tile(sa, a0, a1);
    PV(o, va2, vb2, a0, a1);
    bad |= part_bad(o);
    if (__builtin_expect(b == 0 || __any(bad), 0)) {
        u32x4 kx, vxa, vxb;
#pragma unroll
        for (int i = 0; i < 4; ++i) { kx[i] = mt ? k0[i] : k4[i]; vxa[i] = mt ? va0[i] : va4[i]; vxb[i] = mt ? vb0[i] : vb4[i]; }
        sc = MFMA(asbf(k4), qfo, zero);
        if (i0 == 0) sd = MFMA(asbf(k0), qfo, zero); else sd = bcast(NINF);
#pragma unroll
        for (int i = 0; i < 16; ++i) sc[i] = __builtin_amdgcn_inverse_ballot_w64(diag_mask(i)) ? sc[i] : sd[i];
        mo = max16(sc, NINF); mx = NINF;
        const bool hx = mt == 0 || i0 == 0;
        if (hx) { const f32x16 s = MFMA(asbf(kx), qfx, zero); mx = max16(s, mx); }
        if (i0 <= 1) { const f32x16 s1 = MFMA(asbf(k1), qfo, zero), s2 = MFMA(asbf(k1), qfx, zero); mo = max16(s1, mo); mx = max16(s2, mx); }
        if (i0 <= 2) { const f32x16 s1 = MFMA(asbf(k2), qfo, zero), s2 = MFMA(asbf(k2), qfx, zero); mo = max16(s1, mo); mx = max16(s2, mx); }
        if (i0 <= 3) { const f32x16 s1 = MFMA(asbf(k3), qfo, zero), s2 = MFMA(asbf(k3), qfx, zero); mo = max16(s1, mo); mx = max16(s2, mx); }
        mo = hmerge(mo); mx = hmerge(mx);
        nmo = bcast(-mo); nmx = bcast(-mx);
        o = zero; ox = zero;
#pragma unroll
        for (int i = 0; i < 16; ++i) sc[i] -= mo;
        exp_tile(sc, p0, p1);
#pragma unroll
        for (int i = 0; i < 4; ++i) { d0[i] = p0[i] & dm0[i]; d1[i] = p1[i] & dm1[i]; p0[i] ^= d0[i]; p1[i] ^= d1[i]; }
        PV(o, va4, vb4, d0, d1);
        if (i0 == 0) PV(o, va0, vb0, p0, p1);
        if (hx) { const f32x16 s = MFMA(asbf(kx), qfx, nmx); exp_tile(s, a0, a1); PV(ox, vxa, vxb, a0, a1); }
        if (i0 <= 1) { const f32x16 s = MFMA(asbf(k1), qfx, nmx), t = MFMA(asbf(k1), qfo, nmo); exp_tile(s, a0, a1); PV(ox, va1, vb1, a0, a1); exp_tile(t, a0, a1); PV(o, va1, vb1, a0, a1); }
        if (i0 <= 2) { const f32x16 s = MFMA(asbf(k2), qfx, nmx), t = MFMA(asbf(k2), qfo, nmo); exp_tile(s, a0, a1); PV(ox, va2, vb2, a0, a1); exp_tile(t, a0, a1); PV(o, va2, vb2, a0, a1); }
        if (i0 <= 3) { const f32x16 s = MFMA(asbf(k3), qfx, nmx), t = MFMA(asbf(k3), qfo, nmo); exp_tile(s, a0, a1); PV(ox, va3, vb3, a0, a1); exp_tile(t, a0, a1); PV(o, va3, vb3, a0, a1); }
    }
    *(LAS f32x4*)(exw + lane * 16) = f32x4{ox[0], ox[1], ox[2], ox[3]};
    *(LAS f32x4*)(exw + lane * 16 + 1024) = f32x4{ox[4], ox[5], ox[6], ox[7]};
    *(LAS f32x2*)(lds + L_Y + w * 512 + lane * 8) = f32x2{ox[8], mx};
    BAR();
    {
        const lptr s = lds + L_X + (w ^ 1) * 2048;
        const f32x4 a = *(LAS f32x4*)(s + lane * 16), c = *(LAS f32x4*)(s + lane * 16 + 1024);
        const f32x2 lm = *(LAS f32x2*)(lds + L_Y + (w ^ 1) * 512 + lane * 8);
        const float M = __builtin_fmaxf(mo, lm[1]);
        float fo = __builtin_amdgcn_exp2f(mo - M), fp = __builtin_amdgcn_exp2f(lm[1] - M);
        const float inv = __builtin_amdgcn_rcpf(o[8] * fo + lm[0] * fp);
        fo *= inv; fp *= inv;
        u32x4 t, cc;
        t[0] = pk(o[0] * fo + a[0] * fp, o[1] * fo + a[1] * fp); t[1] = pk(o[2] * fo + a[2] * fp, o[3] * fo + a[3] * fp);
        t[2] = pk(o[4] * fo + c[0] * fp, o[5] * fo + c[1] * fp); t[3] = pk(o[6] * fo + c[2] * fp, o[7] * fo + c[3] * fp);
        ST_SC1(t, recO + lane * 16);
        cc[0] = nonce - csum(t, 0);
        cc[1] = __builtin_amdgcn_update_dpp(0, cc[0], 0x55, 0xF, 0xF, true); cc[2] = __builtin_amdgcn_update_dpp(0, cc[0], 0xAA, 0xF, 0xF, true); cc[3] = __builtin_amdgcn_update_dpp(0, cc[0], 0xFF, 0xF, 0xF, true);
        ST_SC1(cc, (lane & 3) ? WS_OOB : recO + 1024 + lane * 4);
    }
    WAIT_BAR(2);
    const int rh = w & 1, cq = w >> 1;
    bf16x8 wf0[16], wf1[16];
    {
        const int rl = 16 * (g4 >> 1) + 4 * (g4 & 1) + q4;
        lptr e0 = lds + rl * 256 + 32 * ((2 * cq) ^ (rl & 7)) + 8 * p4, e1 = e0 + 8 * HS, e2 = e0 + 16 * HS;
        lptr f0 = lds + rl * 256 + 32 * ((2 * cq + 1) ^ (rl & 7)) + 8 * p4, f1 = f0 + 8 * HS, f2 = f0 + 16 * HS;
        PIN(e1); PIN(e2); PIN(f1); PIN(f2);
#pragma unroll
        for (int t = 0; t < 16; ++t) {
            wf0[t] = trd2((WOS[t] < 8 ? e0 : (WOS[t] < 16 ? e1 : e2)) + (WOS[t] & 7) * HS, 2048);
            wf1[t] = trd2((WOS[t] < 8 ? f0 : (WOS[t] < 16 ? f1 : f2)) + (WOS[t] & 7) * HS, 2048);
        }
    }
    SB();
    {
        const unsigned rO = WS_O + (unsigned)(qb3 * 32 + 4 * w) * REC + lane * 16, dC = 1024u - 12u * lane;
        u32x4 f0, f1, f2, f3; unsigned c0, c1, c2, c3;
        __builtin_amdgcn_s_sleep(OSLEEP);
#pragma unroll 1
        for (int spins = 0; spins < MAXSPIN; ++spins) {
            ALD1(c0, rO + dC); ALD4(f0, rO);
            ALD1(c1, rO + REC + dC); ALD4(f1, rO + REC);
            ALD1(c2, rO + 2 * REC + dC); ALD4(f2, rO + 2 * REC);
            ALD1(c3, rO + 3 * REC + dC); ALD4(f3, rO + 3 * REC);
            asm volatile("s_waitcnt vmcnt(0)" : "+v"(f0), "+v"(c0), "+v"(f1), "+v"(c1), "+v"(f2), "+v"(c2), "+v"(f3), "+v"(c3) :: "memory");
            CSUM4(c0, c1, c2, c3, f0, f1, f2, f3);
            if (!__any((c0 != nO) | (c1 != nO) | (c2 != nO) | (c3 != nO))) break;
            __builtin_amdgcn_s_sleep(2);
        }
        const lptr dd = lds + L_E + (4 * ((w - rot) & 7)) * 1024 + lane * 16;
        *(LAS u32x4*)(dd) = f0; *(LAS u32x4*)(dd + 1024) = f1; *(LAS u32x4*)(dd + 2048) = f2; *(LAS u32x4*)(dd + 3072) = f3;
    }
    BAR();
    f32x4 ac0 = {}, ac1 = {};
#define FOLD(dst, SC) do { const unsigned a0_ = pk(ac0[0] * (SC), ac0[1] * (SC)), a1_ = pk(ac0[2] * (SC), ac0[3] * (SC)), b0_ = pk(ac1[0] * (SC), ac1[1] * (SC)), b1_ = pk(ac1[2] * (SC), ac1[3] * (SC)); \
        auto t0_ = __builtin_amdgcn_permlane16_swap(a0_, b0_, false, false); auto t1_ = __builtin_amdgcn_permlane16_swap(a1_, b1_, false, false); \
        dst[0] = t0_[0]; dst[1] = t1_[0]; dst[2] = t0_[1]; dst[3] = t1_[1]; } while (0)
    {
        const lptr ob2 = lds + L_E + (g4 >> 1) * 1024 + (16 * rh + c16 + 32 * (g4 & 1)) * 16;
#pragma unroll
        for (int t = 0; t < 16; ++t) {
            const bf16x8 a = *(LAS bf16x8*)(ob2 + t * 2048);
            ac0 = MFMA16(wf0[t], a, ac0); ac1 = MFMA16(wf1[t], a, ac1);
        }
        u32x4 t;
        FOLD(t, 1.0f);
        char* po = out + (size_t)(32 * qb3 + 16 * rh + c16) * 1024 + (128 * cb + 32 * cq + 16 * (g4 & 1) + 8 * (g4 >> 1)) * 2;
        asm volatile("global_store_dwordx4 %0, %1, off sc1" :: "v"(po), "v"(t) : "memory");
    }
    if (tid == 0) {
        u64* pg = (u64*)(ws + WS_G + self * 128);
        const u64 val = hsh(gself, 1000);
        asm volatile("global_store_dwordx2 %0, %1, off sc1" :: "v"(pg), "v"(val) : "memory");
    }
}

extern "C" void kernel_launch(void* const* d_in, const int* in_sizes, int n_in,
                              void* d_out, int out_size, void* d_ws, size_t ws_size,
                              hipStream_t stream) {
    (void)hipFuncSetAttribute((const void*)fused_kernel, hipFuncAttributeMaxDynamicSharedMemorySize, LDS_BYTES);
    fused_kernel<<<256, 512, LDS_BYTES, stream>>>((const char*)d_in[0], (const char*)d_in[1], (const char*)d_in[2], (char*)d_out, (char*)d_ws);
}
```

```cpp
#include <hip/hip_runtime.h>
#include <hip/hip_bf16.h>
#include <stdint.h>

typedef __attribute__((ext_vector_type(8))) short bf16x8;
typedef __attribute__((ext_vector_type(4))) short s16x4;
typedef __attribute__((ext_vector_type(16))) float f32x16;
typedef __attribute__((ext_vector_type(4))) float f32x4;
typedef __attribute__((ext_vector_type(2))) float f32x2;
typedef __attribute__((ext_vector_type(4))) unsigned u32x4;
typedef __attribute__((ext_vector_type(2))) unsigned u32x2;
typedef __attribute__((ext_vector_type(2))) __bf16 bf16x2;
typedef __attribute__((ext_vector_type(2))) unsigned short u16x2;
typedef unsigned long long u64;
#define LAS __attribute__((address_space(3)))
#define DI __device__ __forceinline__
typedef LAS char* lptr;

constexpr unsigned REC2 = 2560, REC = 1280;
constexpr unsigned WS_K = 0, WS_V = 1024 * REC2, WS_O = 2048 * REC2, WS_G = WS_O + 2048 * REC, WS_F = WS_G + 256 * 128, WS_T = WS_F + 256 * 128, WS_END = WS_T + 256 * 128;
constexpr unsigned WS_OOB = 0x7FFFF000u;
constexpr unsigned HS = 8192;
constexpr unsigned L_Q = 12 * HS, L_H = 13 * HS, L_K = 14 * HS;
constexpr unsigned L_N = 19 * HS, L_Y = 19 * HS + 4096;
constexpr unsigned L_E = 12 * HS;
constexpr unsigned LDS_BYTES = 20 * HS;
constexpr unsigned CODE_BYTES = 22 * 1024;
constexpr int MAXSPIN = 1 << 15;
constexpr float QSCALE = 0.25f * 1.4426950408889634f;
constexpr unsigned ONES = 0x3F803F80u;
constexpr int WOS[16] = {16, 18, 0, 1, 2, 3, 4, 17, 5, 6, 7, 8, 9, 10, 11, 19};

#define MFMA(a, b, c) __builtin_amdgcn_mfma_f32_32x32x16_bf16((a), (b), (c), 0, 0, 0)
#define MFMA16(a, b, c) __builtin_amdgcn_mfma_f32_16x16x32_bf16((a), (b), (c), 0, 0, 0)
#define SB() __builtin_amdgcn_sched_barrier(0)
#define PIN(x) asm volatile("" : "+v"(x))
#define PINS(x) asm volatile("" : "+s"(x))
#define FENCE() asm volatile("" ::: "memory")
constexpr int wcnt(int vm, int lgkm) { return (vm & 15) | (7 << 4) | (lgkm << 8) | ((vm >> 4) << 14); }
#define WAIT_BAR(N) do { FENCE(); __builtin_amdgcn_s_waitcnt(wcnt(N, 0)); __builtin_amdgcn_s_barrier(); FENCE(); SB(); } while (0)
#define WAIT_BARV(N) do { FENCE(); __builtin_amdgcn_s_waitcnt(wcnt(N, 15)); __builtin_amdgcn_s_barrier(); FENCE(); SB(); } while (0)
#define WAIT_BARL(L) do { FENCE(); __builtin_amdgcn_s_waitcnt(wcnt(63, L)); __builtin_amdgcn_s_barrier(); FENCE(); SB(); } while (0)
#define BARX() do { FENCE(); __builtin_amdgcn_s_barrier(); FENCE(); SB(); } while (0)
#define BAR() WAIT_BAR(63)
#define MX3(a, b, c) __builtin_fmaxf(__builtin_fmaxf((a), (b)), (c))
#define ST_SC1(v, off) __builtin_amdgcn_raw_buffer_store_b128((v), rs, (off), 0, 16)
#define ALD4(dst, off) asm volatile("buffer_load_dwordx4 %0, %1, %2, 0 offen sc1" : "=v"(dst) : "v"(off), "s"(rs) : "memory")
#define ALD4S(dst, off, so) asm volatile("buffer_load_dwordx4 %0, %1, %2, %3 offen sc1" : "=v"(dst) : "v"(off), "s"(rs), "s"(so) : "memory")
#define ALD1S(dst, off, so) asm volatile("buffer_load_dword %0, %1, %2, %3 offen sc1" : "=v"(dst) : "v"(off), "s"(rs), "s"(so) : "memory")

DI unsigned pk(float a, float b) { f32x2 v = {a, b}; bf16x2 r = __builtin_convertvector(v, bf16x2); return __builtin_bit_cast(unsigned, r); }
DI s16x4 trd(lptr p) { return __builtin_amdgcn_ds_read_tr16_b64_v4i16((LAS s16x4*)p); }
DI bf16x8 trd2(lptr p, int off2) { s16x4 a = trd(p), b = trd(p + off2); return __builtin_shufflevector(a, b, 0, 1, 2, 3, 4, 5, 6, 7); }
DI u64 hsh(u64 g, unsigned slot) { return g * 0x9E3779B97F4A7C15ull + (u64)(2u * slot + 1u) * 0xD1B54A32D192ED03ull; }
DI bf16x8 asbf(u32x4 v) { return __builtin_bit_cast(bf16x8, v); }
DI unsigned d2(unsigned a, unsigned k, unsigned c) { unsigned r;
    if (k & 2u) asm("v_pk_mad_u16 %0, %1, %2, %3 op_sel:[1,0,0] op_sel_hi:[0,1,1]" : "=v"(r) : "v"(a), "s"(k), "v"(c));
    else asm("v_pk_mad_u16 %0, %1, %2, %3" : "=v"(r) : "v"(a), "s"(k), "v"(c));
    return r; }
DI unsigned psub(unsigned a, unsigned b) { return __builtin_bit_cast(unsigned, (u16x2)(__builtin_bit_cast(u16x2, a) - __builtin_bit_cast(u16x2, b))); }
DI unsigned csum(u32x4 v, unsigned c) { return d2(v.x, 0x682f978bu, d2(v.y, 0x92e1a2d1u, d2(v.z, 0x4ca14349u, d2(v.w, 0xf6314f97u, c)))); }
DI float max16(const f32x16& s, float m) {
#pragma unroll
    for (int i = 0; i < 16; i += 2) m = MX3(m, s[i], s[i + 1]);
    return m;
}
DI float hmerge(float m) { auto rr = __builtin_amdgcn_permlane32_swap(__float_as_uint(m), __float_as_uint(m), false, false);
    return __builtin_fmaxf(__uint_as_float(rr[0]), __uint_as_float(rr[1])); }
constexpr int crow(int r, int hi) { return (r & 3) + 8 * (r >> 2) + 4 * hi; }
constexpr u64 diag_mask(int reg) { u64 m = 0; for (int l = 0; l < 64; ++l) if (crow(reg, l >> 5) <= (l & 31)) m |= 1ull << l; return m; }
DI f32x16 bcast(float v) { f32x16 r;
#pragma unroll
    for (int i = 0; i < 16; ++i) r[i] = v;
    return r; }
DI f32x16 bcast2(float v) {
    const f32x2 p = {v, v}; f32x2 q[8]; q[0] = p;
#pragma unroll
    for (int i = 1; i < 8; ++i) asm volatile("v_mov_b64 %0, %1" : "=v"(q[i]) : "v"(p));
    f32x16 r;
#pragma unroll
    for (int i = 0; i < 16; ++i) r[i] = q[i >> 1][i & 1];
    return r; }
DI unsigned dppx1(unsigned v) { return __builtin_amdgcn_update_dpp(0, v, 0xB1, 0xF, 0xF, true); }
DI u32x4 fill4(unsigned v) {
    const u64 p = ((u64)v << 32) | v; u32x2 a, b;
    asm volatile("v_mov_b64 %0, %1" : "=v"(a) : "s"(p)); asm volatile("v_mov_b64 %0, %1" : "=v"(b) : "s"(p));
    return u32x4{a[0], a[1], b[0], b[1]}; }
#define CSUM4(e0, e1, e2, e3, v0, v1, v2, v3) do { \
    e0 = d2(v0.w, 0xf6314f97u, e0); e1 = d2(v1.w, 0xf6314f97u, e1); e2 = d2(v2.w, 0xf6314f97u, e2); e3 = d2(v3.w, 0xf6314f97u, e3); asm volatile("" : "+v"(e0), "+v"(e1), "+v"(e2), "+v"(e3)); \
    e0 = d2(v0.z, 0x4ca14349u, e0); e1 = d2(v1.z, 0x4ca14349u, e1); e2 = d2(v2.z, 0x4ca14349u, e2); e3 = d2(v3.z, 0x4ca14349u, e3); asm volatile("" : "+v"(e0), "+v"(e1), "+v"(e2), "+v"(e3)); \
    e0 = d2(v0.y, 0x92e1a2d1u, e0); e1 = d2(v1.y, 0x92e1a2d1u, e1); e2 = d2(v2.y, 0x92e1a2d1u, e2); e3 = d2(v3.y, 0x92e1a2d1u, e3); asm volatile("" : "+v"(e0), "+v"(e1), "+v"(e2), "+v"(e3)); \
    e0 = d2(v0.x, 0x682f978bu, e0); e1 = d2(v1.x, 0x682f978bu, e1); e2 = d2(v2.x, 0x682f978bu, e2); e3 = d2(v3.x, 0x682f978bu, e3); asm volatile("" : "+v"(e0), "+v"(e1), "+v"(e2), "+v"(e3)); } while (0)

#define CSUM2(e0, e1, v0, v1) do { e0 = d2(v0.w, 0xf6314f97u, e0); e1 = d2(v1.w, 0xf6314f97u, e1); e0 = d2(v0.z, 0x4ca14349u, e0); e1 = d2(v1.z, 0x4ca14349u, e1); \
            e0 = d2(v0.y, 0x92e1a2d1u, e0); e1 = d2(v1.y, 0x92e1a2d1u, e1); e0 = d2(v0.x, 0x682f978bu, e0); e1 = d2(v1.x, 0x682f978bu, e1); } while (0)
DI void exp_tile(const f32x16& s, u32x4& p0, u32x4& p1) {
#pragma unroll
    for (int i = 0; i < 4; ++i) p0[i] = pk(__builtin_amdgcn_exp2f(s[2 * i]), __builtin_amdgcn_exp2f(s[2 * i + 1]));
#pragma unroll
    for (int i = 0; i < 4; ++i) p1[i] = pk(__builtin_amdgcn_exp2f(s[8 + 2 * i]), __builtin_amdgcn_exp2f(s[8 + 2 * i + 1]));
}
#define PV(o, va, vb, p0, p1) do { o = MFMA(asbf(va), asbf(p0), o); o = MFMA(asbf(vb), asbf(p1), o); } while (0)
DI u64 part_bad(const f32x16& o, float lo) {
    f32x2 z = (f32x2{o[0], o[1]} + f32x2{o[2], o[3]}) + (f32x2{o[4], o[5]} + f32x2{o[6], o[7]});
    float t;
    asm("v_fma_f32 %0, %1, 0, %2" : "=v"(t) : "v"(z[0]), "v"(o[8]));
    asm("v_fma_f32 %0, %1, 0, %2" : "=v"(t) : "v"(z[1]), "v"(t));
    return __builtin_amdgcn_ballot_w64(!(t < 0x1p100f)) | __builtin_amdgcn_ballot_w64(!(t > lo));
}

__global__ void __launch_bounds__(512, 2)
fused_kernel(const char* __restrict__ x, const char* __restrict__ wqkv, const char* __restrict__ wo, char* __restrict__ out, char* ws) {
    extern __shared__ __attribute__((aligned(16))) char smem[];
    const lptr lds = (lptr)smem;
    const unsigned lds0 = (unsigned)(uintptr_t)smem;
    const char* pc0;
    asm volatile("s_getpc_b64 %0" : "=s"(pc0));
    const int tid = threadIdx.x, lane = tid & 63;
    const int w = __builtin_amdgcn_readfirstlane(tid >> 6);
    unsigned dummy = 0;
    if (w >= 4) {
        __builtin_amdgcn_s_setprio(2);
        const unsigned ln = ((blockIdx.x >> 3) * 4 + (w & 3)) * 2 + lane;
        if (lane < 2 && ln < CODE_BYTES / 128) asm volatile("global_load_dword %0, %1, %2" : "=v"(dummy) : "v"(ln * 128), "s"(pc0) : "memory");
    }
    const int id = blockIdx.x, xcd = id & 7, idx = id >> 3;
    const int b = 8 * (xcd >> 1) + (idx >> 2), g = 4 * (xcd & 1) + (idx & 3);
    const int rho0 = ((idx >> 2) + 2 * (idx & 3)) & 7;
    const int d = w & 3;
    unsigned voffX = (unsigned)(64 * b + 8 * d + (lane >> 3)) * 1024u + (unsigned)((lane & 7) ^ ((4 * d + (lane >> 4)) & 7)) * 16u, voffX2 = voffX + 32768u;
    unsigned voffW = (unsigned)(8 * d + (lane >> 3)) * 3072u + g * 128u + (unsigned)((lane & 7) ^ (((lane >> 4) & 1) << 2)) * 16u, voffW2 = voffW + 32u * 3072u;
    const unsigned ldd = lds0 + (unsigned)d * 1024;
    unsigned sx = (unsigned)((rho0 + 7) & 7) * 128u, sw;
    PIN(voffX); PIN(voffX2); PIN(voffW); PIN(voffW2); PINS(sx);
    SB();
    asm volatile("" :: "s"(x), "s"(wqkv), "s"(wo), "s"(out), "s"(ws));
#define SX(p) (4 * ((p) >> 1) + ((p) & 1))
#define SK(p) (4 * ((p) >> 1) + 2 + ((p) & 1))
#define NEXT "s_add_i32 %0, %0, 0x80\n\ts_and_b32 %0, %0, 0x380\n\t"
#define DMA2(v1, v2, sp, hs) asm volatile("s_add_i32 m0, %3, %4\n\ts_nop 0\n\tglobal_load_lds_dwordx4 %0, %2\n\ts_add_i32 m0, %3, %5\n\ts_nop 0\n\tglobal_load_lds_dwordx4 %1, %2" \
        :: "v"(v1), "v"(v2), "s"(sp), "s"(ldd), "n"((hs) * HS), "n"((hs) * HS + 4096) : "memory", "scc")
#define DMA_X(p) do { asm volatile(NEXT : "+s"(sx) :: "scc"); DMA2(voffX, voffX2, x + sx, SX(p)); } while (0)
#define DMA_W1(matoff, hs) do { sw = sx * 1536u + (matoff); DMA2(voffW, voffW2, wqkv + sw, hs); } while (0)
#define DMA_W(matoff, hs) do { asm volatile(NEXT : "+s"(sx) :: "scc"); DMA_W1(matoff, hs); } while (0)
#define DMA_K(p) DMA_W1(1024, SK(p))
#define DMA_V(p, hs) DMA_W(2048, hs)
#define DMA_Q(p, hs) DMA_W(0, hs)
    if (w < 4) {
        DMA_X(0); DMA_K(0); DMA_X(1); DMA_K(1); DMA_X(2);
    }
    SB();
    const int r = lane & 31, hh = lane >> 5;
    const int mt = w & 1, j = (w >> 1) & 1, kh = w >> 2;
    const int head = 4 * g + 2 * j + kh;
    const int qo = 2 * b + mt;
    const int self = b * 8 + g;
    const int i0 = b < 4 ? 4 - b : 0;
    const __amdgpu_buffer_rsrc_t rs = __builtin_amdgcn_make_buffer_rsrc(ws, 0, WS_END, 0x00020000);
    const unsigned recK = WS_K + (unsigned)((2 * g + j) * 64 + qo) * REC2;
    const unsigned recO = WS_O + (unsigned)(qo * 32 + head) * REC;
    const int cb = g & 3, qb3 = 2 * b + (g >> 2), rot = idx >> 2;
    const int cons = lane < 4 ? (b + 1 + lane) * 8 + g : b * 8 + ((lane - 4) & 7);
    const bool cons_ok = lane < 4 ? (b + 1 + lane < 32) : (lane < 12);
    const unsigned cslot = lane < 4 ? lane : 4 + g;
    u64 gcons;
    const char* pgc = ws + WS_G + (cons_ok && w == 4 ? cons : self) * 128;
#define LOAD_G() asm volatile("global_load_dwordx2 %0, %1, off sc1" : "=v"(gcons) : "v"(pgc) : "memory")
    if (kh) LOAD_G();
    const int c16 = lane & 15, g4 = lane >> 4, q4 = c16 >> 2, p4 = lane & 3;
    u32x4 k0, k1, k2, k3, k4, va0, va1, va2, va3, va4, vb0, vb1, vb2, vb3, vb4;
    unsigned cv0, cv1, cv2, cv3;
    u64 fw;
    bf16x8 qfo, qfx;
    const bool needF = lane < 4 ? (b - 1 - lane >= 0) : (lane < 12);
    const char* pfs = ws + WS_F + (!needF ? self * 128 : (lane < 4 ? (self - 8 - 8 * lane) * 128 + lane * 8 : (b * 8 + lane - 4) * 128 + (4 + g) * 8));
    const unsigned oP = recK + kh * 1024 + lane * 16, oC = recK + 2048 + lane * 8 + kh * 4;
    const unsigned oK0 = i0 <= 0 ? oP - 8 * REC2 : WS_OOB, oK1 = i0 <= 1 ? oP - 6 * REC2 : WS_OOB, oK2 = i0 <= 2 ? oP - 4 * REC2 : WS_OOB, oK3 = i0 <= 3 ? oP - 2 * REC2 : WS_OOB;
    const unsigned cK0 = i0 <= 0 ? oC - 8 * REC2 : WS_OOB, cK1 = i0 <= 1 ? oC - 6 * REC2 : WS_OOB, cK2 = i0 <= 2 ? oC - 4 * REC2 : WS_OOB, cK3 = i0 <= 3 ? oC - 2 * REC2 : WS_OOB;
#define LOAD_VA() do { ALD1S(cv3, cK3, WS_V - WS_K); ALD4S(va3, oK3, WS_V - WS_K); ALD1S(cv0, cK0, WS_V - WS_K); ALD4S(va0, oK0, WS_V - WS_K); } while (0)
#define LOAD_VB() do { ALD1S(cv1, cK1, WS_V - WS_K); ALD4S(va1, oK1, WS_V - WS_K); ALD1S(cv2, cK2, WS_V - WS_K); ALD4S(va2, oK2, WS_V - WS_K); } while (0)
#define WAIT_V(N) asm volatile("s_waitcnt vmcnt(" #N ")" : "+v"(va0), "+v"(va1), "+v"(va2), "+v"(va3), "+v"(cv0), "+v"(cv1), "+v"(cv2), "+v"(cv3) :: "memory")
#define LOAD_F() asm volatile("global_load_dwordx2 %0, %1, off sc1" : "=v"(fw) : "v"(pfs) : "memory")
#define UNF(va, vb) do { _Pragma("unroll") for (int i = 0; i < 4; ++i) { auto t_ = __builtin_amdgcn_permlane16_swap(va[i], vb[i], false, false); va[i] = t_[0]; vb[i] = t_[1]; } PIN(va); PIN(vb); } while (0)
    u32x4 dm0, dm1;
#pragma unroll
    for (int i = 0; i < 4; ++i) {
        dm0[i] = (crow(2 * i, 0) + 4 * hh <= r ? 0xFFFFu : 0u) | (crow(2 * i + 1, 0) + 4 * hh <= r ? 0xFFFF0000u : 0u);
        dm1[i] = (crow(8 + 2 * i, 0) + 4 * hh <= r ? 0xFFFFu : 0u) | (crow(8 + 2 * i + 1, 0) + 4 * hh <= r ? 0xFFFF0000u : 0u);
    }
    PIN(dm0); PIN(dm1);
    const unsigned voffO = (unsigned)(lane >> 4) * 1024u + (unsigned)((lane & 15) ^ ((4 * (w & 1) + (lane >> 4)) << 1)) * 16u;
    u64 gself;
    unsigned nonce;
#define GOT_G() do { FENCE(); PIN(gcons); \
        gself = ((u64)(unsigned)__builtin_amdgcn_readlane((unsigned)(gcons >> 32), 12) << 32) | (unsigned)__builtin_amdgcn_readlane((unsigned)gcons, 12); \
        nonce = (unsigned)(hsh(gself, 77) >> 32); } while (0)
    unsigned fn;
    f32x16 nm;
    const float shift = b == 0 ? 20.f : 70.f;
    u64 tq;
    if (__builtin_expect(w < 4, 1)) {
        DMA_K(2); DMA_X(3); DMA_K(3);
        WAIT_BARV(8);
        DMA_X(4); DMA_K(4); DMA_X(5); DMA_K(5);
        WAIT_BARV(8);
        DMA_X(6); DMA_K(6); DMA_X(7); DMA_K(7);
        WAIT_BARV(8);
        DMA_V(0, 16); LOAD_G(); DMA_V(1, 18); DMA_V(2, 19); DMA_V(3, 0);
        WAIT_BARV(9);
        vb0 = fill4(i0 <= 0 ? ONES : 0u); vb1 = fill4(i0 <= 1 ? ONES : 0u); vb2 = fill4(i0 <= 2 ? ONES : 0u); vb3 = fill4(i0 <= 3 ? ONES : 0u); vb4 = fill4(ONES);
        nm = bcast2(-shift); PIN(nm);
        SB();
        DMA_V(4, 1); DMA_V(5, 2); DMA_V(6, 3); DMA_V(7, 4);
        WAIT_BARV(12);
        u32x4 m0, m1, m2, m3; u32x2 c0, c1, c2, c3;
#define ALD2(dst, off) asm volatile("buffer_load_dwordx2 %0, %1, %2, 0 offen sc1" : "=v"(dst) : "v"(off), "s"(rs) : "memory")
#define ALD4I(dst, off) asm volatile("buffer_load_dwordx4 %0, %1, %2, 0 offen offset:1024 sc1" : "=v"(dst) : "v"(off), "s"(rs) : "memory")
#define KLOAD_A() do { LOAD_F(); ALD2(c3, cK3); ALD4I(m3, oK3); ALD4(k3, oK3); ALD2(c0, cK0); ALD4I(m0, oK0); ALD4(k0, oK0); } while (0)
#define KLOAD_B() do { ALD2(c1, cK1); ALD4I(m1, oK1); ALD4(k1, oK1); ALD2(c2, cK2); ALD4I(m2, oK2); ALD4(k2, oK2); } while (0)
        DMA_Q(0, 17); DMA_Q(1, 5);
        GOT_G();
        unsigned wantF = (unsigned)(hsh(gself, lane) >> 32); PIN(wantF); PINS(nonce);
        unsigned maskF = needF ? ~0u : 0u; PIN(maskF);
        WAIT_BARV(12);
        DMA_Q(2, 6); DMA_Q(3, 7);
        WAIT_BARV(12);
        DMA_Q(4, 8); DMA_Q(5, 9);
        WAIT_BARV(12);
        DMA_Q(6, 10); DMA_Q(7, 11);
        WAIT_BARV(12);
        WAIT_BARV(8);
        __builtin_amdgcn_s_sleep(0);
        KLOAD_A();
        WAIT_BARV(7);
        KLOAD_B();
#define KTILE(N, c, m, k, pos, ln) do { asm volatile("s_waitcnt vmcnt(%3)" : "+v"(k), "+v"(m), "+v"(c) : "n"(N) : "memory"); \
            *(LAS u32x4*)(lds + L_K + d * 4096 + (pos) * 1024 + lane * 16) = m; CSUM2(c[0], c[1], k, m); \
            ek |= (c[0] ^ ln) | (c[1] ^ ln); PIN(ek); } while (0)
#define KVERIFY() do { asm volatile("s_waitcnt vmcnt(12)" : "+v"(fw) :: "memory"); \
            fn = (unsigned)fw & maskF;                   \
            *(LAS unsigned*)(lds + L_N + d * 256 + lane * 4) = fn; \
            unsigned ek = ((unsigned)(fw >> 32) ^ wantF) & maskF; \
            const unsigned s3_ = __builtin_amdgcn_readlane(fn, 0), s0_ = __builtin_amdgcn_readlane(fn, 3), s1_ = __builtin_amdgcn_readlane(fn, 2), s2_ = __builtin_amdgcn_readlane(fn, 1); \
            SB(); \
            KTILE(9, c3, m3, k3, 3, s3_); KTILE(6, c0, m0, k0, 0, s0_); KTILE(3, c1, m1, k1, 1, s1_); KTILE(0, c2, m2, k2, 2, s2_); \
            badk = __builtin_amdgcn_ballot_w64(ek != 0); } while (0)
        u64 badk;
        KVERIFY();
        if (__builtin_expect(badk != 0, 0)) {
#pragma unroll 1
            for (int spins = 0; spins < MAXSPIN; ++spins) {
                __builtin_amdgcn_s_sleep(2);
                KLOAD_A(); KLOAD_B();
                KVERIFY();
                if (!badk) break;
            }
        }
        WAIT_BARL(0);
        asm volatile("s_memrealtime %0" : "=s"(tq) :: "memory");
        qfo = *(LAS bf16x8*)(lds + L_Q + d * 2048 + lane * 16);
        k4 = *(LAS u32x4*)(lds + L_H + d * 2048 + lane * 16);
        va4 = *(LAS u32x4*)(lds + L_H + d * 2048 + 1024 + lane * 16);
    } else {
        lptr wr0 = lds + (8 * hh + q4) * 128 + 64 * (j ^ (q4 >> 1)) + 32 * ((lane >> 4) & 1) + 8 * p4;
        lptr wr1 = wr0 + 8 * HS, wr2 = wr0 + 16 * HS;
        PIN(wr0); PIN(wr1); PIN(wr2);
        lptr xr[4], xs[4];
#pragma unroll
        for (int i = 0; i < 4; ++i) { xr[i] = lds + (32 * mt + r) * 128 + (((2 * i + hh) ^ ((r >> 1) & 7)) * 16); xs[i] = xr[i] + 8 * HS; PIN(xr[i]); PIN(xs[i]); }
        unsigned foff = (w == 4 && lane < 16) ? WS_F + self * 128 + lane * 8 : WS_OOB; PIN(foff);
        unsigned sC = (lane & 1) ? WS_OOB : recK + 2048 + lane * 8; PIN(sC);
        __builtin_amdgcn_s_waitcnt(wcnt(0, 15));
        GOT_G();
        u64 fval = (hsh(gcons, cslot) & 0xFFFFFFFF00000000ull) | nonce; PIN(fval);
        bf16x8 xf[32], wa[8];
        f32x16 accA = {}, accB = {};
#define WRP(hs) (((hs) < 8 ? wr0 : ((hs) < 16 ? wr1 : wr2)) + ((hs) & 7) * HS)
#define RW(u, hs0, hs1) wa[u] = trd2(WRP((u) < 4 ? (hs0) : (hs1)) + ((u) & 3) * 2048, 512)
#define RX(c, u) xf[8 * (c) + (u)] = *(LAS bf16x8*)(((c) < 2 ? xr[(u) & 3] : xs[(u) & 3]) + (4 * ((c) & 1) + ((u) >> 2)) * HS)
#define M1(acc, c, u, SWAP) do { acc = SWAP ? MFMA(xf[8 * (c) + (u)], wa[u], acc) : MFMA(wa[u], xf[8 * (c) + (u)], acc); PIN(acc); } while (0)
#define G1(acc, c, u, SWAP, hs0, hs1, EXTRA) do { M1(acc, c, u, SWAP); RW(u, hs0, hs1); EXTRA; SB(); } while (0)
#define STEP(acc, c, SWAP, hs0, hs1, E0, E1, E2, E3, E4, E5, E6, E7) do { G1(acc, c, 0, SWAP, hs0, hs1, E0); G1(acc, c, 1, SWAP, hs0, hs1, E1); G1(acc, c, 2, SWAP, hs0, hs1, E2); G1(acc, c, 3, SWAP, hs0, hs1, E3); \
        G1(acc, c, 4, SWAP, hs0, hs1, E4); G1(acc, c, 5, SWAP, hs0, hs1, E5); G1(acc, c, 6, SWAP, hs0, hs1, E6); G1(acc, c, 7, SWAP, hs0, hs1, E7); } while (0)
#define KSTAGE(c, hs0, hs1) do { _Pragma("unroll") for (int u = 0; u < 4; ++u) { RW(u, hs0, hs1); RX(c, u); } SB(); \
        _Pragma("unroll") for (int u = 0; u < 4; ++u) { RW(u + 4, hs0, hs1); RX(c, u + 4); SB(); M1(accA, c, u, false); SB(); } \
        _Pragma("unroll") for (int u = 4; u < 8; ++u) M1(accA, c, u, false); SB(); } while (0)
        BARX();
        KSTAGE(0, 2, 3);
        BARX();
        KSTAGE(1, 6, 7);
        BARX();
        KSTAGE(2, 10, 11);
        BARX();
        KSTAGE(3, 14, 15);
        u32x4 ka, cc;
#pragma unroll
        for (int i = 0; i < 4; ++i) { ka[i] = pk(accA[2 * i], accA[2 * i + 1]); k4[i] = pk(accA[8 + 2 * i], accA[8 + 2 * i + 1]); }
        __builtin_amdgcn_raw_buffer_store_b64(__builtin_bit_cast(u32x2, fval), rs, foff, 0, 16);
        ST_SC1(ka, oP - 1024); ST_SC1(k4, oP);
        SB();
        cc[0] = cc[1] = 0; CSUM2(cc[0], cc[1], ka, k4);
        cc[0] = psub(nonce, cc[0]); cc[1] = psub(nonce, cc[1]);
        cc[2] = dppx1(cc[0]); cc[3] = dppx1(cc[1]); PIN(cc); PIN(ka); PIN(k4);
        ST_SC1(cc, sC);
        BARX();
#pragma unroll
        for (int u = 0; u < 8; ++u) RW(u, 16, 18);
        BARX();
        STEP(accB, 0, true, 19, 0, , , , , , , , );
        BARX();
        STEP(accB, 1, true, 1, 2, , , , , , , , );
        BARX();
        STEP(accB, 2, true, 3, 4, , , , , , , , );
        BARX();
        STEP(accB, 3, true, 17, 5, , , , , , , , );
        BARX();
        accA = f32x16{};
        u32x4 ra;
        STEP(accA, 0, false, 6, 7, ,
             _Pragma("unroll") for (int i = 0; i < 4; ++i) ra[i] = pk(accB[2 * i], accB[2 * i + 1]); PIN(ra),
             _Pragma("unroll") for (int i = 0; i < 4; ++i) va4[i] = pk(accB[8 + 2 * i], accB[8 + 2 * i + 1]); PIN(va4),
             UNF(ra, va4),
             cc[0] = psub(nonce, csum(ra, 0)); PIN(cc[0]),
             cc[1] = psub(nonce, csum(va4, 0)); PIN(cc[1]),
             cc[2] = dppx1(cc[0]); cc[3] = dppx1(cc[1]); PIN(cc), );
        BARX();
        STEP(accA, 1, false, 8, 9,
             *(LAS u32x4*)(lds + L_H + d * 2048 + lane * 16) = ka, ,
             *(LAS u32x4*)(lds + L_H + d * 2048 + 1024 + lane * 16) = ra, ,
             vb4 = fill4(ONES),
             vb0 = fill4(i0 <= 0 ? ONES : 0u); vb1 = fill4(i0 <= 1 ? ONES : 0u),
             vb2 = fill4(i0 <= 2 ? ONES : 0u); vb3 = fill4(i0 <= 3 ? ONES : 0u), );
        STEP(accA, 2, false, 10, 11, ST_SC1(ra, oP - 1024 + (WS_V - WS_K)), , ST_SC1(va4, oP + (WS_V - WS_K)), , ST_SC1(cc, sC + (WS_V - WS_K)), , , );
#pragma unroll
        for (int u = 0; u < 8; ++u) M1(accA, 3, u, false);
        SB();
        {
            u32x4 t, tp;
#pragma unroll
            for (int i = 0; i < 4; ++i) { tp[i] = pk(accA[2 * i] * QSCALE, accA[2 * i + 1] * QSCALE); t[i] = pk(accA[8 + 2 * i] * QSCALE, accA[8 + 2 * i + 1] * QSCALE); }
            qfo = asbf(t);
            *(LAS u32x4*)(lds + L_Q + d * 2048 + lane * 16) = tp;
            *(LAS u32x4*)(lds + L_Q + d * 2048 + 1024 + lane * 16) = t;
        }
        nm = bcast2(-shift); PIN(nm);
        WAIT_BARL(0);
        asm volatile("s_memrealtime %0" : "=s"(tq) :: "memory");
        __builtin_amdgcn_s_setprio(1);
        k3 = *(LAS u32x4*)(lds + L_K + d * 4096 + 3072 + lane * 16);
        k0 = *(LAS u32x4*)(lds + L_K + d * 4096 + lane * 16);
        k1 = *(LAS u32x4*)(lds + L_K + d * 4096 + 1024 + lane * 16);
        k2 = *(LAS u32x4*)(lds + L_K + d * 4096 + 2048 + lane * 16);
        fn = *(LAS unsigned*)(lds + L_N + d * 256 + lane * 4);
        asm volatile("s_waitcnt vmcnt(7)" : "+v"(dummy) :: "memory");
    }
    qfx = *(LAS bf16x8*)(lds + L_Q + (d ^ 1) * 2048 + kh * 1024 + lane * 16);
    const f32x16 zero = {};
    const float NINF = -__builtin_inff();
    f32x16 sa, sb;
    sa = MFMA(asbf(k3), qfo, nm); PIN(sa);
    const __amdgpu_buffer_rsrc_t ro = __builtin_amdgcn_make_buffer_rsrc((char*)wo + (4 * w) * 1024 + cb * 256, 0, 512 * 1024 - ((4 * w) * 1024 + cb * 256), 0x00020000);
    unsigned so = (unsigned)(2 * rot - 1) * 32768u;
    const unsigned ldw = lds0 + (unsigned)w * 1024;
#define DMA_O(h) asm volatile("s_add_i32 m0, %2, %3\n\ts_add_i32 %0, %0, 0x8000\n\ts_and_b32 %0, %0, 0x7ffff\n\tbuffer_load_dwordx4 %1, %4, %0 offen lds" \
        : "+s"(so) : "v"(voffO), "s"(ldw), "n"(WOS[h] * HS), "s"(ro) : "memory", "scc")
    float mo = shift, mx = shift;
    f32x16 ox = {}, o = {};
    u32x4 p0, p1, d0, d1, a0, a1, b0, b1;
    f32x16 sc = MFMA(asbf(k4), qfo, nm), sd = MFMA(asbf(k0), qfo, nm);
    PIN(sc); PIN(sd);
    sb = MFMA(asbf(k3), qfx, nm); PIN(sb);
    LOAD_VA();
    asm volatile("s_waitcnt lgkmcnt(0)" : "+s"(tq) :: "memory");
    const unsigned tq0 = (unsigned)tq;
    exp_tile(sa, a0, a1); PIN(a0); PIN(a1); SB();
    UNF(va4, vb4);
    const unsigned n0 = __builtin_amdgcn_readlane(fn, 3), n1 = __builtin_amdgcn_readlane(fn, 2), n2 = __builtin_amdgcn_readlane(fn, 1), n3 = __builtin_amdgcn_readlane(fn, 0);
    const unsigned nO = __builtin_amdgcn_readlane(fn, 4 + w);
    if (mt) { sa = MFMA(asbf(k0), qfx, nm); PIN(sa); } else { sa = MFMA(asbf(k4), qfx, nm); PIN(sa); FENCE(); }
    LOAD_VB();
    DMA_O(0); DMA_O(1);
#pragma unroll
    for (int i = 0; i < 16; ++i) sc[i] = __builtin_amdgcn_inverse_ballot_w64(diag_mask(i)) ? sc[i] : sd[i];
    exp_tile(sc, p0, p1);
#pragma unroll
    for (int i = 0; i < 4; ++i) { d0[i] = p0[i] & dm0[i]; d1[i] = p1[i] & dm1[i]; p0[i] ^= d0[i]; p1[i] ^= d1[i]; }
    PIN(p0); PIN(p1); PIN(d0); PIN(d1); SB();
    PV(o, va4, vb4, d0, d1); PIN(o);
    __builtin_amdgcn_raw_buffer_store_b64(u32x2{tq0, ~tq0}, rs, (w == 4 && lane < 16) ? WS_T + self * 128 + lane * 8 : WS_OOB, 0, 16);
    DMA_O(2); DMA_O(3);
    exp_tile(sb, b0, b1); PIN(b0); PIN(b1); SB();
    sb = MFMA(asbf(k1), qfx, nm); PIN(sb);
    DMA_O(4); DMA_O(5);
    exp_tile(sa, d0, d1); PIN(d0); PIN(d1); SB();
    WAIT_V(7);
    CSUM4(cv0, cv1, cv2, cv3, va0, va1, va2, va3);
    if (__builtin_expect(__any((cv0 != n0) | (cv1 != n1) | (cv2 != n2) | (cv3 != n3)), 0)) {
#pragma unroll 1
        for (int spins = 0; spins < MAXSPIN; ++spins) {
            __builtin_amdgcn_s_sleep(2);
            LOAD_VA(); LOAD_VB(); WAIT_V(0);
            CSUM4(cv0, cv1, cv2, cv3, va0, va1, va2, va3);
            if (!__any((cv0 != n0) | (cv1 != n1) | (cv2 != n2) | (cv3 != n3))) break;
        }
    }
    UNF(va3, vb3); UNF(va0, vb0);
    SB();
    const lptr exw = lds + (kh ? L_K + d * 4096 : L_H + d * 2048) + lane * 16;
    const lptr eyw = lds + L_Y + w * 512 + lane * 8;
    if (mt) { PV(ox, va0, vb0, d0, d1); PIN(ox); } else { PV(ox, va4, vb4, d0, d1); PIN(ox); FENCE(); }
    PV(o, va3, vb3, a0, a1); PIN(o); DMA_O(6); DMA_O(7);
    UNF(va1, vb1); UNF(va2, vb2); SB();
    sa = MFMA(asbf(k2), qfx, nm); PIN(sa);
    PV(ox, va3, vb3, b0, b1); PIN(ox); PV(o, va0, vb0, p0, p1); PIN(o);
    DMA_O(8); DMA_O(9);
    exp_tile(sb, b0, b1); PIN(b0); PIN(b1); SB();
    __builtin_amdgcn_s_setprio(0);
    sb = MFMA(asbf(k1), qfo, nm); PIN(sb);
    PV(ox, va1, vb1, b0, b1); PIN(ox);
    DMA_O(10); DMA_O(11);
    u32x2 tp;
    { unsigned to = WS_T + (unsigned)(b * 8 + w) * 128; PIN(to); ALD2(tp, to); }
    exp_tile(sa, a0, a1); PIN(a0); PIN(a1); SB();
    sa = MFMA(asbf(k2), qfo, nm); PIN(sa);
    PV(ox, va2, vb2, a0, a1); PIN(ox);
    DMA_O(12); DMA_O(13); DMA_O(14);
    exp_tile(sb, b0, b1); PIN(b0); PIN(b1); SB();
    PV(o, va1, vb1, b0, b1); PIN(o);
    *(LAS f32x4*)(exw) = f32x4{ox[0], ox[1], ox[2], ox[3]};
    *(LAS f32x4*)(exw + 1024) = f32x4{ox[4], ox[5], ox[6], ox[7]};
    *(LAS f32x2*)(eyw) = f32x2{ox[8], qo == 1 ? NINF : mx};
    u64 bad = part_bad(ox, qo == 1 ? -1.f : 0x1p-100f);
    PINS(bad);
    SB();
    exp_tile(sa, a0, a1);
    PV(o, va2, vb2, a0, a1);
    bad |= part_bad(o, 0x1p-100f);
    if (__builtin_expect(bad != 0, 0)) {
        u32x4 kx, vxa, vxb;
#pragma unroll
        for (int i = 0; i < 4; ++i) { kx[i] = mt ? k0[i] : k4[i]; vxa[i] = mt ? va0[i] : va4[i]; vxb[i] = mt ? vb0[i] : vb4[i]; }
        sc = MFMA(asbf(k4), qfo, zero);
        if (i0 == 0) sd = MFMA(asbf(k0), qfo, zero); else sd = bcast(NINF);
#pragma unroll
        for (int i = 0; i < 16; ++i) sc[i] = __builtin_amdgcn_inverse_ballot_w64(diag_mask(i)) ? sc[i] : sd[i];
        mo = max16(sc, NINF); mx = NINF;
        const bool hx = mt == 0 || i0 == 0;
        if (hx) { const f32x16 s = MFMA(asbf(kx), qfx, zero); mx = max16(s, mx); }
        if (i0 <= 1) { const f32x16 s1 = MFMA(asbf(k1), qfo, zero), s2 = MFMA(asbf(k1), qfx, zero); mo = max16(s1, mo); mx = max16(s2, mx); }
        if (i0 <= 2) { const f32x16 s1 = MFMA(asbf(k2), qfo, zero), s2 = MFMA(asbf(k2), qfx, zero); mo = max16(s1, mo); mx = max16(s2, mx); }
        if (i0 <= 3) { const f32x16 s1 = MFMA(asbf(k3), qfo, zero), s2 = MFMA(asbf(k3), qfx, zero); mo = max16(s1, mo); mx = max16(s2, mx); }
        mo = hmerge(mo); mx = hmerge(mx);
        const f32x16 nmo = bcast(-mo), nmx = bcast(-mx);
        o = zero; ox = zero;
#pragma unroll
        for (int i = 0; i < 16; ++i) sc[i] -= mo;
        exp_tile(sc, p0, p1);
#pragma unroll
        for (int i = 0; i < 4; ++i) { d0[i] = p0[i] & dm0[i]; d1[i] = p1[i] & dm1[i]; p0[i] ^= d0[i]; p1[i] ^= d1[i]; }
        PV(o, va4, vb4, d0, d1);
        if (i0 == 0) PV(o, va0, vb0, p0, p1);
        if (hx) { const f32x16 s = MFMA(asbf(kx), qfx, nmx); exp_tile(s, a0, a1); PV(ox, vxa, vxb, a0, a1); }
        if (i0 <= 1) { const f32x16 s = MFMA(asbf(k1), qfx, nmx), t = MFMA(asbf(k1), qfo, nmo); exp_tile(s, a0, a1); PV(ox, va1, vb1, a0, a1); exp_tile(t, a0, a1); PV(o, va1, vb1, a0, a1); }
        if (i0 <= 2) { const f32x16 s = MFMA(asbf(k2), qfx, nmx), t = MFMA(asbf(k2), qfo, nmo); exp_tile(s, a0, a1); PV(ox, va2, vb2, a0, a1); exp_tile(t, a0, a1); PV(o, va2, vb2, a0, a1); }
        if (i0 <= 3) { const f32x16 s = MFMA(asbf(k3), qfx, nmx), t = MFMA(asbf(k3), qfo, nmo); exp_tile(s, a0, a1); PV(ox, va3, vb3, a0, a1); exp_tile(t, a0, a1); PV(o, va3, vb3, a0, a1); }
        *(LAS f32x4*)(exw) = f32x4{ox[0], ox[1], ox[2], ox[3]};
        *(LAS f32x4*)(exw + 1024) = f32x4{ox[4], ox[5], ox[6], ox[7]};
        *(LAS f32x2*)(eyw) = f32x2{ox[8], mx};
    }
    BAR();
    {
        const lptr s = lds + (kh ? L_K + (d ^ 1) * 4096 : L_H + (d ^ 1) * 2048) + lane * 16;
        const f32x2 lm = *(LAS f32x2*)(lds + L_Y + (w ^ 1) * 512 + lane * 8);
        const f32x4 a = *(LAS f32x4*)(s), c = *(LAS f32x4*)(s + 1024);
        SB();
        const float M = __builtin_fmaxf(mo, lm[1]);
        float fo = __builtin_amdgcn_exp2f(mo - M), fp = __builtin_amdgcn_exp2f(lm[1] - M);
        const float inv = __builtin_amdgcn_rcpf(o[8] * fo + lm[0] * fp);
        fo *= inv; fp *= inv;
        u32x4 t, cc;
        t[0] = pk(o[0] * fo + a[0] * fp, o[1] * fo + a[1] * fp); t[1] = pk(o[2] * fo + a[2] * fp, o[3] * fo + a[3] * fp);
        t[2] = pk(o[4] * fo + c[0] * fp, o[5] * fo + c[1] * fp); t[3] = pk(o[6] * fo + c[2] * fp, o[7] * fo + c[3] * fp);
        ST_SC1(t, recO + lane * 16);
        cc[0] = psub(nonce, csum(t, 0));
        cc[1] = __builtin_amdgcn_update_dpp(0, cc[0], 0x55, 0xF, 0xF, true); cc[2] = __builtin_amdgcn_update_dpp(0, cc[0], 0xAA, 0xF, 0xF, true); cc[3] = __builtin_amdgcn_update_dpp(0, cc[0], 0xFF, 0xF, 0xF, true);
        ST_SC1(cc, (lane & 3) ? WS_OOB : recO + 1024 + lane * 4);
    }
    WAIT_BAR(2);
    PIN(tp);
    DMA_O(15);
    const int rh = w & 1, cq = w >> 1;
    bf16x8 wf0[16], wf1[16];
    const int rl = 16 * (g4 >> 1) + 4 * (g4 & 1) + q4;
    lptr e0 = lds + rl * 256 + 32 * ((2 * cq) ^ (rl & 7)) + 8 * p4, e1 = e0 + 8 * HS, e2 = e0 + 16 * HS;
    lptr f0 = lds + rl * 256 + 32 * ((2 * cq + 1) ^ (rl & 7)) + 8 * p4, f1 = f0 + 8 * HS, f2 = f0 + 16 * HS;
    PIN(e1); PIN(e2); PIN(f1); PIN(f2);
#define RWO(t) do { wf0[t] = trd2((WOS[t] < 8 ? e0 : (WOS[t] < 16 ? e1 : e2)) + (WOS[t] & 7) * HS, 2048); \
        wf1[t] = trd2((WOS[t] < 8 ? f0 : (WOS[t] < 16 ? f1 : f2)) + (WOS[t] & 7) * HS, 2048); } while (0)
#pragma unroll
    for (int t = 0; t < 15; ++t) RWO(t);
    SB();
    char* po = out + (size_t)(32 * qb3 + 16 * rh + c16) * 1024 + (128 * cb + 32 * cq + 16 * (g4 & 1) + 8 * (g4 >> 1)) * 2;
    lptr ob2 = lds + L_E + (g4 >> 1) * 1024 + (16 * rh + c16 + 32 * (g4 & 1)) * 16;
    {
        unsigned rO = WS_O + (unsigned)(qb3 * 32 + 4 * w) * REC + lane * 16, rC = rO + 1024u - 12u * lane;
        lptr dd = lds + L_E + (4 * ((w - rot) & 7)) * 1024 + lane * 16;
        PIN(rO); PIN(rC); PIN(dd); PIN(po); PIN(ob2);
        u32x4 g0, g1, g2, g3; unsigned c0, c1, c2, c3, eo;
        __builtin_amdgcn_s_sleep(0);
        {
            const int late = (int)(__builtin_amdgcn_readfirstlane(tp[0]) - tq0);
            const bool ok = __builtin_amdgcn_readfirstlane(tp[0] ^ tp[1]) == ~0u && late > -400 && late < 400;
            int n = ok ? (late * 7) >> 5 : 30;
#pragma unroll 1
            for (; n > 0; --n) __builtin_amdgcn_s_sleep(1);
        }
#define OLD(c, g, k) do { asm volatile("buffer_load_dword %0, %1, %2, 0 offen offset:%3 sc1" : "=v"(c) : "v"(rC), "s"(rs), "n"((k) * REC) : "memory"); \
        asm volatile("buffer_load_dwordx4 %0, %1, %2, 0 offen offset:%3 sc1" : "=v"(g) : "v"(rO), "s"(rs), "n"((k) * REC) : "memory"); } while (0)
#define OLOADS() do { OLD(c0, g0, 0); OLD(c1, g1, 1); OLD(c2, g2, 2); OLD(c3, g3, 3); } while (0)
#define OT2(N, ca, ga, cb_, gb, k) do { asm volatile("s_waitcnt vmcnt(%4)" : "+v"(ca), "+v"(ga), "+v"(cb_), "+v"(gb) : "n"(N) : "memory"); \
        *(LAS u32x4*)(dd + (k) * 1024) = ga; *(LAS u32x4*)(dd + (k) * 1024 + 1024) = gb; CSUM2(ca, cb_, ga, gb); eo |= (ca ^ nO) | (cb_ ^ nO); PIN(eo); } while (0)
#define OVERIFY() do { eo = 0; OT2(4, c0, g0, c1, g1, 0); OT2(0, c2, g2, c3, g3, 2); } while (0)
        OLOADS();
        SB();
        OVERIFY();
        if (__builtin_expect(__any(eo != 0), 0)) {
#pragma unroll 1
            for (int spins = 0; spins < MAXSPIN; ++spins) {
                __builtin_amdgcn_s_sleep(2);
                OLOADS();
                OVERIFY();
                if (!__any(eo != 0)) break;
            }
        }
    }
    BAR();
    f32x4 ac0 = {}, ac1 = {};
#define FOLD(dst, SC) do { const unsigned a0_ = pk(ac0[0] * (SC), ac0[1] * (SC)), a1_ = pk(ac0[2] * (SC), ac0[3] * (SC)), b0_ = pk(ac1[0] * (SC), ac1[1] * (SC)), b1_ = pk(ac1[2] * (SC), ac1[3] * (SC)); \
        auto t0_ = __builtin_amdgcn_permlane16_swap(a0_, b0_, false, false); auto t1_ = __builtin_amdgcn_permlane16_swap(a1_, b1_, false, false); \
        dst[0] = t0_[0]; dst[1] = t1_[0]; dst[2] = t0_[1]; dst[3] = t1_[1]; } while (0)
    {
        bf16x8 a[16];
#pragma unroll
        for (int t = 0; t < 16; ++t) a[t] = *(LAS bf16x8*)(ob2 + t * 2048);
        RWO(15);
        SB();
#pragma unroll
        for (int t = 0; t < 16; ++t) { ac0 = MFMA16(wf0[t], a[t], ac0); ac1 = MFMA16(wf1[t], a[t], ac1); }
        u32x4 t;
        FOLD(t, 1.0f);
        asm volatile("global_store_dwordx4 %0, %1, off sc1\n\ts_nop 1" :: "v"(po), "v"(t) : "memory");
    }
    {
        const u64 val = hsh(gself, 1000);
        __builtin_amdgcn_raw_buffer_store_b64(__builtin_bit_cast(u32x2, val), rs, (w == 0 && lane < 16) ? WS_G + self * 128 + lane * 8 : WS_OOB, 0, 16);
    }
}

extern "C" void kernel_launch(void* const* d_in, const int* in_sizes, int n_in,
                              void* d_out, int out_size, void* d_ws, size_t ws_size,
                              hipStream_t stream) {
    (void)hipFuncSetAttribute((const void*)fused_kernel, hipFuncAttributeMaxDynamicSharedMemorySize, LDS_BYTES);
    fused_kernel<<<256, 512, LDS_BYTES, stream>>>((const char*)d_in[0], (const char*)d_in[1], (const char*)d_in[2], (char*)d_out, (char*)d_ws);
}
```
